```python
import torch

def _softmax(x, axis=-1):
    return torch.softmax(x, dim=axis)
def _gelu(x):
    return torch.nn.functional.gelu(x, approximate="tanh")
def _silu(x):
    return torch.nn.functional.silu(x)
def _sigmoid(x):
    return torch.sigmoid(x)
def _layernorm(x, axis=-1, eps=1e-5):
    m = x.mean(dim=axis, keepdim=True)
    v = x.var(dim=axis, keepdim=True, unbiased=False)
    return (x - m) / torch.sqrt(v + eps)
def _scatter_add(tgt, idx, src, axis=0):
    return tgt.clone().index_add_(axis, idx, src)
def _segment_sum(src, seg, n):
    out = torch.zeros((n,) + src.shape[1:], dtype=src.dtype)
    return out.index_add_(0, seg, src)
def _pad(x, pw):
    flat = tuple(v for pair in reversed(pw) for v in pair)
    return torch.nn.functional.pad(x, flat)

kernel_name = 'sequential_preLN__sliding_local_dilated__geglu_ffn__prompt'

def setup_inputs():

    _w = (lambda a: (lambda t: t.to(torch.bfloat16) if t.is_floating_point() else t)(torch.as_tensor(a)))
    def _A_setup():
        B, S, D = 1, 2048, 1024
        return {
            "x": _w((lambda *s: torch.randn(*s, dtype=torch.bfloat16) * 0.05)(B, S, D)),
            "wqkv": _w((lambda *s: torch.randn(*s, dtype=torch.bfloat16) * 0.05)(D, 3 * D)),
            "wo": _w((lambda *s: torch.randn(*s, dtype=torch.bfloat16) * 0.05)(D, D)),
        }
    _A_in = _A_setup()

    def _B_setup():
        B, S, D, F = 1, 2048, 1024, 3584
        return {
            "x": _w((lambda *s: torch.randn(*s, dtype=torch.bfloat16) * 0.05)(B, S, D)),
            "w_gate": _w((lambda *s: torch.randn(*s, dtype=torch.bfloat16) * 0.05)(D, F)),
            "w_up": _w((lambda *s: torch.randn(*s, dtype=torch.bfloat16) * 0.05)(D, F)),
            "w_down": _w((lambda *s: torch.randn(*s, dtype=torch.bfloat16) * 0.05)(F, D)),
        }
    _B_in = _B_setup()

    return {
        "A_x": _A_in["x"],
        "A_wqkv": _A_in["wqkv"],
        "A_wo": _A_in["wo"],
        "B_x": _B_in["x"],
        "B_w_gate": _B_in["w_gate"],
        "B_w_up": _B_in["w_up"],
        "B_w_down": _B_in["w_down"],
    }

def reference(A_x, A_wqkv, A_wo, B_x, B_w_gate, B_w_up, B_w_down):
    def _A_ref(x, wqkv, wo):
        B, S, D = x.shape
        H = 32; Dh = D // H
        W, dil = 8, 2
        qkv = (lambda x, *d: x.permute(*d))((x @ wqkv).reshape(B, S, 3, H, Dh), 2, 0, 3, 1, 4)
        q, k, v = qkv[0], qkv[1], qkv[2]
        scale = Dh ** -0.5
        pos = torch.arange(S)
        off = torch.arange(W) * dil
        idx = pos[:, None] - off[None, :]
        mask = idx >= 0
        idx_c = torch.where(mask, idx, 0).reshape(-1)
        kg = k[:, :, idx_c, :].reshape(B, H, S, W, Dh)
        vg = v[:, :, idx_c, :].reshape(B, H, S, W, Dh)
        logits = torch.einsum("bhsd,bhswd->bhsw", q, kg) * scale
        logits = torch.where(mask[None, None, :, :], logits, -1e9)
        attn = _softmax(logits, axis=-1)
        o = torch.einsum("bhsw,bhswd->bhsd", attn, vg)
        return (lambda x, *d: x.permute(*d))(o, 0, 2, 1, 3).reshape(B, S, D) @ wo

    def _B_ref(x, w_gate, w_up, w_down):
        return (_gelu(x @ w_gate) * (x @ w_up)) @ w_down

    x = A_x
    h = x + _A_ref(_layernorm(x, axis=-1), A_wqkv, A_wo)
    h2 = h + _B_ref(_layernorm(h, axis=-1), B_w_gate, B_w_up, B_w_down)
    return h2
```

```cpp
#include <hip/hip_runtime.h>
#include <hip/hip_bf16.h>

typedef unsigned short u16;
typedef __attribute__((ext_vector_type(8))) __bf16 bf16x8;
typedef __attribute__((ext_vector_type(2))) __bf16 bf16x2;
typedef __attribute__((ext_vector_type(4))) short s16x4;
typedef __attribute__((ext_vector_type(4))) float f32x4;
typedef __attribute__((ext_vector_type(2))) float f32x2;
typedef __attribute__((ext_vector_type(4))) int i32x4;
typedef __attribute__((ext_vector_type(4))) unsigned u32x4;
typedef __attribute__((ext_vector_type(2))) unsigned u32x2;
#define LAS __attribute__((address_space(3)))
#define DEVI __device__ __forceinline__

constexpr int S_ = 2048, D_ = 1024, F_ = 3584;

template <int N> struct IC { static constexpr int v = N; };

DEVI i32x4 make_srd(const void* p, unsigned bytes) {
  unsigned long long a = (unsigned long long)p;
  i32x4 r;
  r.x = (int)(unsigned)a; r.y = (int)((unsigned)(a >> 32) & 0xffffu); r.z = (int)bytes; r.w = 0x00020000;
  return r;
}
DEVI void dma16(unsigned lds_addr, unsigned voff, i32x4 srd, unsigned soff) {
  asm volatile("s_mov_b32 m0, %0\n\ts_nop 0\n\tbuffer_load_dwordx4 %1, %2, %3 offen lds"
               :: "s"(lds_addr), "v"(voff), "s"(srd), "s"(soff));
}
template <int N> DEVI void wait_vm() { asm volatile("s_waitcnt vmcnt(%0)" :: "n"(N) : "memory"); }
DEVI void wait_lgkm0() { asm volatile("s_waitcnt lgkmcnt(0)" ::: "memory"); }
DEVI void bar() { asm volatile("" ::: "memory"); __builtin_amdgcn_s_barrier(); asm volatile("" ::: "memory"); }

DEVI bf16x8 lds_b128(const LAS char* p) { return *(const LAS bf16x8*)p; }
DEVI bf16x8 lds_tr2(const LAS char* p) {
  s16x4 a = __builtin_amdgcn_ds_read_tr16_b64_v4i16((LAS s16x4*)p);
  s16x4 b = __builtin_amdgcn_ds_read_tr16_b64_v4i16((LAS s16x4*)(p + 512));
  return __builtin_bit_cast(bf16x8, __builtin_shufflevector(a, b, 0, 1, 2, 3, 4, 5, 6, 7));
}
DEVI f32x4 mfma(bf16x8 a, bf16x8 b, f32x4 c) { return __builtin_amdgcn_mfma_f32_16x16x32_bf16(a, b, c, 0, 0, 0); }
DEVI unsigned pk_bf16(float lo, float hi) { bf16x2 v = {(__bf16)lo, (__bf16)hi}; return __builtin_bit_cast(unsigned, v); }
DEVI float bf_lo(unsigned u) { return __uint_as_float(u << 16); }
DEVI float bf_hi(unsigned u) { return __uint_as_float(u & 0xffff0000u); }
DEVI float rbf(float x) { return (float)(__bf16)x; }
DEVI float red16_add(float x) { auto s = __builtin_amdgcn_permlane16_swap(__float_as_uint(x), __float_as_uint(x), false, false); return __uint_as_float(s[0]) + __uint_as_float(s[1]); }
DEVI float red32_add(float x) { auto s = __builtin_amdgcn_permlane32_swap(__float_as_uint(x), __float_as_uint(x), false, false); return __uint_as_float(s[0]) + __uint_as_float(s[1]); }
DEVI float red16_max(float x) { auto s = __builtin_amdgcn_permlane16_swap(__float_as_uint(x), __float_as_uint(x), false, false); return fmaxf(__uint_as_float(s[0]), __uint_as_float(s[1])); }
DEVI float red32_max(float x) { auto s = __builtin_amdgcn_permlane32_swap(__float_as_uint(x), __float_as_uint(x), false, false); return fmaxf(__uint_as_float(s[0]), __uint_as_float(s[1])); }

__global__ __launch_bounds__(256) void k0_ln(const u16* __restrict__ x, u16* __restrict__ xn) {
  const int lane = threadIdx.x & 63, row = blockIdx.x * 4 + (threadIdx.x >> 6);
  const u32x4* src = (const u32x4*)(x + (size_t)row * D_);
  u32x4 a = src[lane], b = src[lane + 64];
  float v[16];
#pragma unroll
  for (int i = 0; i < 4; ++i) { v[2 * i] = bf_lo(a[i]); v[2 * i + 1] = bf_hi(a[i]); v[8 + 2 * i] = bf_lo(b[i]); v[8 + 2 * i + 1] = bf_hi(b[i]); }
  float s = 0.f;
#pragma unroll
  for (int i = 0; i < 16; ++i) s += v[i];
#pragma unroll
  for (int o = 1; o < 64; o <<= 1) s += __shfl_xor(s, o);
  const float mean = s * (1.f / D_);
  float q = 0.f;
#pragma unroll
  for (int i = 0; i < 16; ++i) { float d = v[i] - mean; q += d * d; }
#pragma unroll
  for (int o = 1; o < 64; o <<= 1) q += __shfl_xor(q, o);
  const float mb = rbf(mean), den = rbf(sqrtf(rbf(rbf(q * (1.f / D_)) + 1e-5f)));
  u32x4 oa, ob;
#pragma unroll
  for (int i = 0; i < 4; ++i) {
    oa[i] = pk_bf16(rbf(v[2 * i] - mb) / den, rbf(v[2 * i + 1] - mb) / den);
    ob[i] = pk_bf16(rbf(v[8 + 2 * i] - mb) / den, rbf(v[8 + 2 * i + 1] - mb) / den);
  }
  u32x4* dst = (u32x4*)(xn + (size_t)row * D_);
  dst[lane] = oa; dst[lane + 64] = ob;
}

constexpr int K1_STAGE = 18 * 1024 + 24 * 1024;
constexpr int K1_NS = 3;
struct K1Frag { bf16x8 x[4], xh, wq[2], wk[2], wv[2]; };

__global__ __launch_bounds__(256, 1) void k1_qkv_attn(const u16* __restrict__ xn, const u16* __restrict__ wqkv, u16* __restrict__ o) {
  __shared__ __attribute__((aligned(16))) char smem_[K1_STAGE * K1_NS];
  LAS char* smem = (LAS char*)smem_;
  const unsigned lds0 = (unsigned)(size_t)smem;
  const int tid = threadIdx.x, lane = tid & 63, w = __builtin_amdgcn_readfirstlane(tid >> 6);
  const int hh = w & 1, rh = w >> 1, g = lane >> 4, c = lane & 15;
  const int bid = blockIdx.x, xcd = bid & 7, idx = bid >> 3;
  const int tm = 4 * (xcd >> 1) + (idx & 3), hp = 8 * (xcd & 1) + (idx >> 2);
  const int r0 = tm * 128;
  const i32x4 srdA = make_srd(xn, S_ * D_ * 2), srdW = make_srd(wqkv, D_ * 3 * D_ * 2);
  const int lr = lane >> 3, lc = lane & 7;
  const unsigned voffA = (unsigned)((8 * w + lr) * (D_ * 2) + ((lc ^ ((4 * w + (lane >> 4)) & 7)) << 4));
  const int fW = ((lane >> 4) & 1) | ((w & 1) << 1);
  const unsigned voffW = (unsigned)((8 * w + lr) * (3 * D_ * 2) + (((((lc >> 1) ^ fW) << 1) | (lc & 1)) << 4));
  const unsigned sA = (unsigned)r0 * (D_ * 2);
  const unsigned sH = (unsigned)(r0 >= 16 ? r0 - 16 : 0) * (D_ * 2);
  const unsigned sW = (unsigned)hp * 128;
  auto issue = [&](int t, int slot) {
    const unsigned lb = lds0 + slot * K1_STAGE + w * 1024;
#pragma unroll
    for (int j = 0; j < 4; ++j) dma16(lb + j * 4096, voffA, srdA, sA + (unsigned)(32 * j) * (D_ * 2) + t * 128);
#pragma unroll
    for (int s = 0; s < 3; ++s)
#pragma unroll
      for (int j = 0; j < 2; ++j) dma16(lb + 18432 + s * 8192 + j * 4096, voffW, srdW, sW + (unsigned)(t * 64 + 32 * j) * (3 * D_ * 2) + s * (D_ * 2));
    if (w < 2) dma16(lb + 16384, voffA, srdA, sH + t * 128);
  };
  const int sw = (lane >> 1) & 7;
  const int aoff0 = (64 * rh + c) * 128 + ((g ^ sw) << 4), hoff0 = (128 + c) * 128 + ((g ^ sw) << 4);
  const int q4 = c >> 2, p4 = lane & 3, fR = ((q4 >> 1) & 1) | ((g & 1) << 1);
  const int woff0 = 18432 + (8 * g + q4) * 128 + (((2 * hh + rh) ^ fR) << 5) + 8 * p4;
  auto load = [&](K1Frag& f, int slot, int ks) {
    const LAS char* b = smem + slot * K1_STAGE;
    const LAS char* pa = b + (aoff0 ^ (ks << 6));
    const LAS char* ph = b + (hoff0 ^ (ks << 6));
    const LAS char* pw0 = b + woff0 + ks * 4096;
    const LAS char* pw1 = b + (woff0 ^ 32) + ks * 4096;
    f.wq[0] = lds_tr2(pw0); f.wq[1] = lds_tr2(pw1);
#pragma unroll
    for (int m = 0; m < 4; ++m) f.x[m] = lds_b128(pa + m * 2048);
    f.wk[0] = lds_tr2(pw0 + 8192); f.wk[1] = lds_tr2(pw1 + 8192);
    f.xh = lds_b128(ph);
    f.wv[0] = lds_tr2(pw0 + 16384); f.wv[1] = lds_tr2(pw1 + 16384);
  };
  f32x4 aq[4][2], ak[4][2], av[4][2], ahk, ahv;
#pragma unroll
  for (int m = 0; m < 4; ++m)
#pragma unroll
    for (int s = 0; s < 2; ++s) { aq[m][s] = (f32x4){0, 0, 0, 0}; ak[m][s] = (f32x4){0, 0, 0, 0}; av[m][s] = (f32x4){0, 0, 0, 0}; }
  ahk = (f32x4){0, 0, 0, 0}; ahv = (f32x4){0, 0, 0, 0};
  auto mma = [&](const K1Frag& f) {
#pragma unroll
    for (int s = 0; s < 2; ++s)
#pragma unroll
      for (int m = 0; m < 4; ++m) aq[m][s] = mfma(f.wq[s], f.x[m], aq[m][s]);
#pragma unroll
    for (int s = 0; s < 2; ++s)
#pragma unroll
      for (int m = 0; m < 4; ++m) ak[m][s] = mfma(f.wk[s], f.x[m], ak[m][s]);
    ahk = mfma(f.wk[0], f.xh, ahk);
#pragma unroll
    for (int s = 0; s < 2; ++s)
#pragma unroll
      for (int m = 0; m < 4; ++m) av[m][s] = mfma(f.x[m], f.wv[s], av[m][s]);
    ahv = mfma(f.xh, f.wv[0], ahv);
  };
  constexpr int NT = D_ / 64;
  K1Frag fa, fb;
  issue(0, 0); issue(1, 1); issue(2, 2);
  wait_vm<20>(); bar();
  load(fa, 0, 0);
  auto step = [&](int t, auto slot_c, auto vm_c, auto iss_c, auto last_c) {
    constexpr int slot = decltype(slot_c)::v;
    load(fb, slot, 1);
    mma(fa);
    if constexpr (!decltype(last_c)::v) {
      wait_lgkm0(); wait_vm<decltype(vm_c)::v>(); bar();
      if constexpr (decltype(iss_c)::v) issue(t + 3, slot);
      load(fa, (slot + 1) % K1_NS, 0);
    }
    mma(fb);
  };
  for (int t = 0; t < 12; t += 3) {
    step(t, IC<0>{}, IC<10>{}, IC<1>{}, IC<0>{});
    step(t + 1, IC<1>{}, IC<10>{}, IC<1>{}, IC<0>{});
    step(t + 2, IC<2>{}, IC<10>{}, IC<1>{}, IC<0>{});
  }
  step(12, IC<0>{}, IC<10>{}, IC<1>{}, IC<0>{});
  step(13, IC<1>{}, IC<10>{}, IC<0>{}, IC<0>{});
  step(14, IC<2>{}, IC<0>{}, IC<0>{}, IC<0>{});
  step(15, IC<0>{}, IC<0>{}, IC<0>{}, IC<1>{});
  static_assert(NT == 16, "");
  u32x2 qp[4][2], kp[4][2], vp[4][2];
#pragma unroll
  for (int m = 0; m < 4; ++m)
#pragma unroll
    for (int s = 0; s < 2; ++s) {
      qp[m][s] = (u32x2){pk_bf16(aq[m][s][0], aq[m][s][1]), pk_bf16(aq[m][s][2], aq[m][s][3])};
      kp[m][s] = (u32x2){pk_bf16(ak[m][s][0], ak[m][s][1]), pk_bf16(ak[m][s][2], ak[m][s][3])};
      vp[m][s] = (u32x2){pk_bf16(av[m][s][0], av[m][s][1]), pk_bf16(av[m][s][2], av[m][s][3])};
    }
  const u32x2 hkp = {pk_bf16(ahk[0], ahk[1]), pk_bf16(ahk[2], ahk[3])}, hvp = {pk_bf16(ahv[0], ahv[1]), pk_bf16(ahv[2], ahv[3])};
  LAS char* ex = smem + K1_STAGE;
  auto exa = [&](int h_, int r_, int kind, int b) { return (LAS u32x2*)(ex + ((((h_ * 2 + r_) * 2 + kind) * 2 + b) * 64 + lane) * 8); };
  *exa(hh, 0, 0, rh) = hkp; *exa(hh, 0, 1, rh) = hvp;
  if (rh == 0) {
    *exa(hh, 1, 0, 0) = kp[3][0]; *exa(hh, 1, 0, 1) = kp[3][1];
    *exa(hh, 1, 1, 0) = vp[3][0]; *exa(hh, 1, 1, 1) = vp[3][1];
  }
  wait_lgkm0(); bar();
  u32x2 kprev[2], vprev[2];
#pragma unroll
  for (int s = 0; s < 2; ++s) { kprev[s] = *exa(hh, rh, 0, s ^ rh); vprev[s] = *exa(hh, rh, 1, s ^ rh); }
  const float scale = 0.17677669529663687f, L2E = 1.4426950408889634f;
  const int dim_off = (((g & 1) ^ rh) << 4) + ((g >> 1) << 3);
#pragma unroll
  for (int m = 0; m < 4; ++m) {
    const u32x2 k0a = m ? kp[m - 1][0] : kprev[0], k0b = m ? kp[m - 1][1] : kprev[1];
    const bf16x8 kf0 = __builtin_bit_cast(bf16x8, (u32x4){k0a[0], k0a[1], k0b[0], k0b[1]});
    const bf16x8 kf1 = __builtin_bit_cast(bf16x8, (u32x4){kp[m][0][0], kp[m][0][1], kp[m][1][0], kp[m][1][1]});
    const bf16x8 qf = __builtin_bit_cast(bf16x8, (u32x4){qp[m][0][0], qp[m][0][1], qp[m][1][0], qp[m][1][1]});
    f32x4 s0 = mfma(kf0, qf, (f32x4){0, 0, 0, 0});
    f32x4 s1 = mfma(kf1, qf, (f32x4){0, 0, 0, 0});
    const int qrow = r0 + 64 * rh + 16 * m + c;
    float l[8]; float mx = -3.0e38f;
#pragma unroll
    for (int r = 0; r < 4; ++r) {
      const int d0 = c + 16 - 4 * g - r, d1 = c - 4 * g - r;
      const bool v0 = ((d0 & 1) == 0) && (d0 <= 14) && (qrow - d0 >= 0);
      const bool v1 = ((d1 & 1) == 0) && (d1 >= 0);
      l[r] = v0 ? rbf(rbf(s0[r]) * scale) : -3.0e38f;
      l[4 + r] = v1 ? rbf(rbf(s1[r]) * scale) : -3.0e38f;
      mx = fmaxf(mx, fmaxf(l[r], l[4 + r]));
    }
    mx = red32_max(red16_max(mx));
    float sum = 0.f;
#pragma unroll
    for (int r = 0; r < 8; ++r) { l[r] = __builtin_amdgcn_exp2f((l[r] - mx) * L2E); sum += l[r]; }
    sum = red32_add(red16_add(sum));
    const float inv = 1.0f / sum;
    const bf16x8 pf = __builtin_bit_cast(bf16x8, (u32x4){pk_bf16(l[0] * inv, l[1] * inv), pk_bf16(l[2] * inv, l[3] * inv), pk_bf16(l[4] * inv, l[5] * inv), pk_bf16(l[6] * inv, l[7] * inv)});
    u32x2 op[2];
#pragma unroll
    for (int s = 0; s < 2; ++s) {
      const u32x2 v0 = m ? vp[m - 1][s] : vprev[s];
      const bf16x8 vf = __builtin_bit_cast(bf16x8, (u32x4){v0[0], v0[1], vp[m][s][0], vp[m][s][1]});
      const f32x4 ot = mfma(vf, pf, (f32x4){0, 0, 0, 0});
      op[s] = (u32x2){pk_bf16(ot[0], ot[1]), pk_bf16(ot[2], ot[3])};
    }
    auto e0 = __builtin_amdgcn_permlane16_swap(op[0][0], op[1][0], false, false);
    auto e1 = __builtin_amdgcn_permlane16_swap(op[0][1], op[1][1], false, false);
    *(u32x4*)(o + (size_t)qrow * D_ + (hp * 2 + hh) * 32 + dim_off) = (u32x4){e0[0], e1[0], e0[1], e1[1]};
  }
}

constexpr int G64_STAGE = 24 * 1024, G64_NS = 6;
struct G64Frag { bf16x8 x[2][2], w[4][2]; };

template <int K, bool STATS>
__global__ __launch_bounds__(256, 1) void k_gemm_n1024(const u16* __restrict__ A, const u16* __restrict__ W, const u16* __restrict__ R,
                                                        u16* __restrict__ O, f32x2* __restrict__ stats) {
  __shared__ __attribute__((aligned(16))) char smem_[G64_STAGE * G64_NS];
  LAS char* smem = (LAS char*)smem_;
  const unsigned lds0 = (unsigned)(size_t)smem;
  const int tid = threadIdx.x, lane = tid & 63, w = __builtin_amdgcn_readfirstlane(tid >> 6);
  const int g = lane >> 4, c = lane & 15;
  const int bid = blockIdx.x, xcd = bid & 7, idx = bid >> 3;
  const int tm = 4 * (xcd >> 1) + (idx & 3), tn = 8 * (xcd & 1) + (idx >> 2);
  const int r0 = tm * 128, c0 = tn * 64;
  const i32x4 srdA = make_srd(A, S_ * K * 2), srdW = make_srd(W, K * D_ * 2);
  const int ecol = c0 + ((g & 1) << 4) + ((g >> 1) << 3);
  u32x4 res[2][2];
#pragma unroll
  for (int m = 0; m < 2; ++m)
#pragma unroll
    for (int jp = 0; jp < 2; ++jp) res[m][jp] = *(const u32x4*)(R + (size_t)(r0 + 32 * w + 16 * m + c) * D_ + ecol + 32 * jp);
  const int lr = lane >> 3, lc = lane & 7;
  const unsigned voffA = (unsigned)((8 * w + lr) * (K * 2) + ((lc ^ ((4 * w + (lane >> 4)) & 7)) << 4));
  const int fW = ((lane >> 4) & 1) | ((w & 1) << 1);
  const unsigned voffW = (unsigned)((8 * w + lr) * (D_ * 2) + (((((lc >> 1) ^ fW) << 1) | (lc & 1)) << 4));
  const unsigned sA = (unsigned)r0 * (K * 2), sW = (unsigned)c0 * 2;
  auto issue = [&](int t, int slot) {
    const unsigned lb = lds0 + slot * G64_STAGE + w * 1024;
#pragma unroll
    for (int j = 0; j < 2; ++j) dma16(lb + 16384 + j * 4096, voffW, srdW, sW + (unsigned)(t * 64 + 32 * j) * (D_ * 2));
#pragma unroll
    for (int j = 0; j < 4; ++j) dma16(lb + j * 4096, voffA, srdA, sA + (unsigned)(32 * j) * (K * 2) + t * 128);
  };
  const int sw = (lane >> 1) & 7;
  const int aoff0 = (32 * w + c) * 128 + ((g ^ sw) << 4);
  const int q4 = c >> 2, p4 = lane & 3, fR = ((q4 >> 1) & 1) | ((g & 1) << 1);
  const int woff0 = 16384 + (8 * g + q4) * 128 + (fR << 5) + 8 * p4;
  auto load = [&](G64Frag& f, int slot) {
    const LAS char* b = smem + slot * G64_STAGE;
#pragma unroll
    for (int ks = 0; ks < 2; ++ks) {
#pragma unroll
      for (int m = 0; m < 2; ++m) f.x[m][ks] = lds_b128(b + (aoff0 ^ (ks << 6)) + m * 2048);
#pragma unroll
      for (int j = 0; j < 4; ++j) f.w[j][ks] = lds_tr2(b + (woff0 ^ (j << 5)) + ks * 4096);
    }
  };
  f32x4 acc[2][4];
#pragma unroll
  for (int m = 0; m < 2; ++m)
#pragma unroll
    for (int j = 0; j < 4; ++j) acc[m][j] = (f32x4){0, 0, 0, 0};
  auto mma = [&](const G64Frag& f) {
#pragma unroll
    for (int ks = 0; ks < 2; ++ks)
#pragma unroll
      for (int j = 0; j < 4; ++j)
#pragma unroll
        for (int m = 0; m < 2; ++m) acc[m][j] = mfma(f.w[j][ks], f.x[m][ks], acc[m][j]);
  };
  constexpr int NT = K / 64;
  G64Frag fr[2];
#pragma unroll
  for (int s = 0; s < G64_NS; ++s) issue(s, s);
  wait_vm<30>(); bar();
  load(fr[0], 0);
  auto step = [&](int t, auto slot_c, auto vm_c, auto iss_c, auto last_c) {
    constexpr int slot = decltype(slot_c)::v;
    if constexpr (!decltype(last_c)::v) {
      wait_lgkm0(); wait_vm<decltype(vm_c)::v>(); bar();
      if constexpr (decltype(iss_c)::v) issue(t + G64_NS, slot);
      load(fr[(slot + 1) & 1], (slot + 1) % G64_NS);
    }
    mma(fr[slot & 1]);
  };
  constexpr int NG = (NT - G64_NS) / G64_NS;
  for (int t = 0; t < NG * G64_NS; t += G64_NS) {
    step(t + 0, IC<0>{}, IC<24>{}, IC<1>{}, IC<0>{});
    step(t + 1, IC<1>{}, IC<24>{}, IC<1>{}, IC<0>{});
    step(t + 2, IC<2>{}, IC<24>{}, IC<1>{}, IC<0>{});
    step(t + 3, IC<3>{}, IC<24>{}, IC<1>{}, IC<0>{});
    step(t + 4, IC<4>{}, IC<24>{}, IC<1>{}, IC<0>{});
    step(t + 5, IC<5>{}, IC<24>{}, IC<1>{}, IC<0>{});
  }
  auto tail = [&](auto self, auto t_c) {
    constexpr int t = decltype(t_c)::v;
    if constexpr (t < NT) {
      constexpr int issued_max = (t + G64_NS - 1 < NT - 1) ? t + G64_NS - 1 : NT - 1;
      constexpr int vm = (issued_max - (t + 1)) > 0 ? (issued_max - (t + 1)) * 6 : 0;
      step(t, IC<t % G64_NS>{}, IC<vm>{}, IC<(t + G64_NS < NT) ? 1 : 0>{}, IC<(t == NT - 1) ? 1 : 0>{});
      self(self, IC<t + 1>{});
    }
  };
  tail(tail, IC<NG * G64_NS>{});
#pragma unroll
  for (int m = 0; m < 2; ++m) {
    const int row = r0 + 32 * w + 16 * m + c;
    float hv[2][8];
#pragma unroll
    for (int jp = 0; jp < 2; ++jp) {
      const f32x4 a0 = acc[m][2 * jp], a1 = acc[m][2 * jp + 1];
      auto e0 = __builtin_amdgcn_permlane16_swap(pk_bf16(a0[0], a0[1]), pk_bf16(a1[0], a1[1]), false, false);
      auto e1 = __builtin_amdgcn_permlane16_swap(pk_bf16(a0[2], a0[3]), pk_bf16(a1[2], a1[3]), false, false);
      const unsigned e[4] = {e0[0], e1[0], e0[1], e1[1]};
      u32x4 ov;
#pragma unroll
      for (int i = 0; i < 4; ++i) {
        ov[i] = pk_bf16(bf_lo(e[i]) + bf_lo(res[m][jp][i]), bf_hi(e[i]) + bf_hi(res[m][jp][i]));
        hv[jp][2 * i] = bf_lo(ov[i]); hv[jp][2 * i + 1] = bf_hi(ov[i]);
      }
      *(u32x4*)(O + (size_t)row * D_ + ecol + 32 * jp) = ov;
    }
    if constexpr (STATS) {
      float s = 0.f;
#pragma unroll
      for (int i = 0; i < 8; ++i) s += hv[0][i] + hv[1][i];
      s = red32_add(red16_add(s));
      const float mean = s * (1.f / 64.f);
      float q = 0.f;
#pragma unroll
      for (int i = 0; i < 8; ++i) { float d0 = hv[0][i] - mean, d1 = hv[1][i] - mean; q += d0 * d0 + d1 * d1; }
      q = red32_add(red16_add(q));
      if (g == 0) stats[row * 16 + tn] = (f32x2){mean, q};
    }
  }
}

constexpr int K3_AH = 16384;
constexpr int K3_UOFF = 14336 + 128;
constexpr int K3_BB = 28928;
constexpr int K3_B0 = 4 * K3_AH;
constexpr int K3_TAB = K3_B0 + 3 * K3_BB;
constexpr int K3_LDS = K3_TAB + 2048 + 4 * 224 * 4;

DEVI bf16x8 lds_tr2_k3(const LAS char* p) {
  s16x4 a = __builtin_amdgcn_ds_read_tr16_b64_v4i16((LAS s16x4*)p);
  s16x4 b = __builtin_amdgcn_ds_read_tr16_b64_v4i16((LAS s16x4*)(p + 896));
  return __builtin_bit_cast(bf16x8, __builtin_shufflevector(a, b, 0, 1, 2, 3, 4, 5, 6, 7));
}

__global__ __launch_bounds__(512, 2) void k3_geglu(const u16* __restrict__ h, const u16* __restrict__ wg, const u16* __restrict__ wu,
                                                    const f32x2* __restrict__ stats, u16* __restrict__ act) {
  __shared__ __attribute__((aligned(16))) char smem_[K3_LDS];
  LAS char* smem = (LAS char*)smem_;
  const unsigned lds0 = (unsigned)(size_t)smem;
  const int tid = threadIdx.x, lane = tid & 63, w = __builtin_amdgcn_readfirstlane(tid >> 6);
  const int grp = w >> 2, w4 = w & 3, wm = (w >> 1) & 1, wn = w & 1, g = lane >> 4, c = lane & 15;
  const int bid = blockIdx.x, xcd = bid & 7, idx = bid >> 3;
  const int tm = 4 * (xcd & 1) + (idx & 3), tn = 8 * (xcd >> 1) + (idx >> 2);
  const int r0 = tm * 256, f0 = tn * 112;
  f32x4 st[4];
  {
    const f32x4* sp = (const f32x4*)(stats + (size_t)(r0 + (tid >> 1)) * 16 + (tid & 1) * 8);
#pragma unroll
    for (int i = 0; i < 4; ++i) st[i] = sp[i];
  }
  const i32x4 srdA = make_srd(h, S_ * D_ * 2), srdG = make_srd(wg, D_ * F_ * 2), srdU = make_srd(wu, D_ * F_ * 2);
  const i32x4 srdB = grp ? srdU : srdG;
  const int lr = lane >> 3, lc = lane & 7;
  const unsigned voffA = (unsigned)((8 * w4 + lr) * (D_ * 2) + ((lc ^ ((4 * w4 + (lane >> 4)) & 7)) << 4));
  unsigned voffB[4];
#pragma unroll
  for (int j = 0; j < 4; ++j) {
    int ch = (w4 + 4 * j) * 64 + lane; if (ch > 895) ch = 895;
    const int k = ch / 14, pos = ch - k * 14;
    voffB[j] = (unsigned)(k * (F_ * 2) + ((pos ^ ((k >> 3) & 1)) << 4));
  }
  const unsigned sA = (unsigned)r0 * (D_ * 2), sB = (unsigned)f0 * 2;
  auto issueA = [&](int t, int half, int buf) {
    const unsigned lb = lds0 + (half * 2 + buf) * K3_AH + w4 * 1024;
#pragma unroll
    for (int j = 0; j < 4; ++j) dma16(lb + j * 4096, voffA, srdA, sA + (unsigned)(128 * half + 32 * j) * (D_ * 2) + t * 128);
  };
  auto issueB = [&](int t, int buf) {
    const unsigned lb = lds0 + K3_B0 + buf * K3_BB + grp * K3_UOFF + w4 * 1024;
#pragma unroll
    for (int j = 0; j < 3; ++j) dma16(lb + j * 4096, voffB[j], srdB, sB + (unsigned)(t * 64) * (F_ * 2));
    if (w4 < 2) dma16(lb + 3 * 4096, voffB[3], srdB, sB + (unsigned)(t * 64) * (F_ * 2));
  };
  const int sw = (lane >> 1) & 7;
  const int aoff0 = grp * 2 * K3_AH + (64 * wm + c) * 128 + ((g ^ sw) << 4);
  const int q4 = c >> 2, p4 = lane & 3;
  const int boffE = K3_B0 + (8 * g + q4) * 224 + (p4 >> 1) * K3_UOFF + 8 * (p4 & 1);
  int boff[7];
#pragma unroll
  for (int j = 0; j < 7; ++j) boff[j] = boffE + (((7 * wn + j) ^ (g & 1)) << 4);
  bf16x8 fx[4][2], fw[7][2];
  auto load = [&](int abuf, int bbuf) {
#pragma unroll
    for (int ks = 0; ks < 2; ++ks) {
#pragma unroll
      for (int j = 0; j < 7; ++j) fw[j][ks] = lds_tr2_k3(smem + boff[j] + bbuf * K3_BB + ks * 7168);
#pragma unroll
      for (int m = 0; m < 4; ++m) fx[m][ks] = lds_b128(smem + (aoff0 ^ (ks << 6)) + abuf * K3_AH + m * 2048);
    }
  };
  f32x4 acc[4][7];
#pragma unroll
  for (int m = 0; m < 4; ++m)
#pragma unroll
    for (int j = 0; j < 7; ++j) acc[m][j] = (f32x4){0, 0, 0, 0};
  float cs[7];
#pragma unroll
  for (int j = 0; j < 7; ++j) cs[j] = 0.f;
  auto mma = [&]() {
    __builtin_amdgcn_s_setprio(1);
#pragma unroll
    for (int ks = 0; ks < 2; ++ks)
#pragma unroll
      for (int j = 0; j < 7; ++j)
#pragma unroll
        for (int m = 0; m < 4; ++m) acc[m][j] = mfma(fw[j][ks], fx[m][ks], acc[m][j]);
    __builtin_amdgcn_s_setprio(0);
  };
  auto colsum = [&]() {
    const bf16x2 one = {(__bf16)1.0f, (__bf16)1.0f};
#pragma unroll
    for (int j = 0; j < 7; ++j)
#pragma unroll
      for (int ks = 0; ks < 2; ++ks)
#pragma unroll
        for (int i = 0; i < 4; ++i) cs[j] = __builtin_amdgcn_fdot2_f32_bf16((bf16x2){fw[j][ks][2 * i], fw[j][ks][2 * i + 1]}, one, cs[j], false);
  };
  constexpr int NT = D_ / 64;
  const int csel = grp * 2 + wm;
  if (grp == 0) { issueA(0, 0, 0); issueB(0, 0); issueA(0, 1, 0); issueB(1, 1); }
  else          { issueB(0, 0); issueA(1, 0, 1); issueB(1, 1); }
  {
    float ms = (st[0][0] + st[0][2]) + (st[1][0] + st[1][2]) + (st[2][0] + st[2][2]) + (st[3][0] + st[3][2]);
    ms += __shfl_xor(ms, 1);
    const float mean = ms * (1.f / 16.f);
    float q = 0.f;
#pragma unroll
    for (int i = 0; i < 4; ++i) { float d0 = st[i][0] - mean, d1 = st[i][2] - mean; q += st[i][1] + st[i][3] + 64.f * (d0 * d0 + d1 * d1); }
    q += __shfl_xor(q, 1);
    const float den = rbf(sqrtf(rbf(rbf(q * (1.f / D_)) + 1e-5f)));
    if ((tid & 1) == 0) *(LAS f32x2*)(smem + K3_TAB + (tid >> 1) * 8) = (f32x2){rbf(mean), 1.0f / den};
  }
  wait_vm<7>();
  wait_lgkm0(); bar();
  if (grp == 1) bar();
  auto tile = [&](int t, auto ab_c, auto bb_c, auto mode_c) {
    constexpr int ab = decltype(ab_c)::v, bb = decltype(bb_c)::v, mode = decltype(mode_c)::v;
    load(ab, bb);
    if (grp == 0) {
      if constexpr (mode <= 1) issueA(t + 1, 1, ab ^ 1);
      if constexpr (mode == 0) issueB(t + 2, (bb + 2) % 3);
    } else {
      if constexpr (mode == 0) { issueA(t + 2, 0, ab); issueB(t + 2, (bb + 2) % 3); }
    }
    wait_lgkm0();
    if ((t & 3) == csel) colsum();
    if constexpr (mode == 2) {
#pragma unroll
      for (int j = 0; j < 7; ++j) {
        const float s = red32_add(red16_add(cs[j]));
        if (g == 0) *(LAS float*)(smem + K3_TAB + 2048 + (csel * 224 + (7 * wn + j) * 16 + c) * 4) = s;
      }
      wait_lgkm0();
    }
    if (grp == 0) { if constexpr (mode == 0) wait_vm<10>(); else if constexpr (mode == 1) wait_vm<7>(); else wait_vm<0>(); }
    else          { if constexpr (mode == 0) wait_vm<7>(); else wait_vm<0>(); }
    bar();
    mma();
    if (grp == 0) { if constexpr (mode == 0) wait_vm<7>(); else if constexpr (mode == 1) wait_vm<4>(); }
    if constexpr (mode == 2) { if (grp == 0) bar(); } else bar();
  };
  for (int t = 0; t < 12; t += 6) {
    tile(t + 0, IC<0>{}, IC<0>{}, IC<0>{});
    tile(t + 1, IC<1>{}, IC<1>{}, IC<0>{});
    tile(t + 2, IC<0>{}, IC<2>{}, IC<0>{});
    tile(t + 3, IC<1>{}, IC<0>{}, IC<0>{});
    tile(t + 4, IC<0>{}, IC<1>{}, IC<0>{});
    tile(t + 5, IC<1>{}, IC<2>{}, IC<0>{});
  }
  tile(12, IC<0>{}, IC<0>{}, IC<0>{});
  tile(13, IC<1>{}, IC<1>{}, IC<0>{});
  tile(14, IC<0>{}, IC<2>{}, IC<1>{});
  tile(15, IC<1>{}, IC<0>{}, IC<2>{});
  static_assert(NT == 16, "");
  const float C0 = -2.0f * 1.4426950408889634f * 0.7978845608028654f, C1 = C0 * 0.044715f;
  f32x2 rs[4];
#pragma unroll
  for (int m = 0; m < 4; ++m) rs[m] = *(const LAS f32x2*)(smem + K3_TAB + (128 * grp + 64 * wm + 16 * m + c) * 8);
  const int mrow = (g >> 1) + 2 * (g & 1);
#pragma unroll
  for (int j = 0; j < 7; ++j) {
    const LAS f32x4* cp = (const LAS f32x4*)(smem + K3_TAB + 2048 + ((7 * wn + j) * 16 + 4 * g) * 4);
    const f32x4 csum = cp[0] + cp[56] + cp[112] + cp[168];
    u32x2 pr[2];
#pragma unroll
    for (int mp = 0; mp < 2; ++mp) {
      f32x4 x = (acc[2 * mp][j] - rs[2 * mp][0] * csum) * rs[2 * mp][1];
      f32x4 y = (acc[2 * mp + 1][j] - rs[2 * mp + 1][0] * csum) * rs[2 * mp + 1][1];
      float a[4];
#pragma unroll
      for (int r = 0; r < 4; ++r) {
        auto s = __builtin_amdgcn_permlane32_swap(__float_as_uint(x[r]), __float_as_uint(y[r]), false, false);
        const float gt = __uint_as_float(s[0]), up = __uint_as_float(s[1]);
        const float e = __builtin_amdgcn_exp2f(gt * (C0 + C1 * gt * gt));
        a[r] = gt * up * __builtin_amdgcn_rcpf(1.0f + e);
      }
      pr[mp] = (u32x2){pk_bf16(a[0], a[1]), pk_bf16(a[2], a[3])};
    }
    auto e0 = __builtin_amdgcn_permlane16_swap(pr[0][0], pr[1][0], false, false);
    auto e1 = __builtin_amdgcn_permlane16_swap(pr[0][1], pr[1][1], false, false);
    const int row = r0 + 128 * grp + 64 * wm + 16 * mrow + c;
    *(u32x4*)(act + (size_t)row * F_ + f0 + (7 * wn + j) * 8) = (u32x4){e0[0], e1[0], e0[1], e1[1]};
  }
}

extern "C" void kernel_launch(void* const* d_in, const int* in_sizes, int n_in, void* d_out, int out_size, void* d_ws, size_t ws_size,
                              hipStream_t stream) {
  const u16* x = (const u16*)d_in[0];
  const u16* wqkv = (const u16*)d_in[1];
  const u16* wo = (const u16*)d_in[2];
  const u16* wg = (const u16*)d_in[4];
  const u16* wu = (const u16*)d_in[5];
  const u16* wd = (const u16*)d_in[6];
  u16* out = (u16*)d_out;
  char* ws = (char*)d_ws;
  u16* xn = (u16*)(ws);
  u16* o = (u16*)(ws + (4u << 20));
  u16* h = (u16*)(ws + (8u << 20));
  f32x2* stats = (f32x2*)(ws + (12u << 20));
  u16* act = (u16*)(ws + (16u << 20));
  k0_ln<<<S_ / 4, 256, 0, stream>>>(x, xn);
  k1_qkv_attn<<<256, 256, 0, stream>>>(xn, wqkv, o);
  k_gemm_n1024<D_, true><<<256, 256, 0, stream>>>(o, wo, x, h, stats);
  k3_geglu<<<256, 512, 0, stream>>>(h, wg, wu, stats, act);
  k_gemm_n1024<F_, false><<<256, 256, 0, stream>>>(act, wd, h, out, nullptr);
}
```

```cpp
#include <hip/hip_runtime.h>
#include <hip/hip_bf16.h>

typedef unsigned short u16;
typedef __attribute__((ext_vector_type(8))) __bf16 bf16x8;
typedef __attribute__((ext_vector_type(2))) __bf16 bf16x2;
typedef __attribute__((ext_vector_type(4))) short s16x4;
typedef __attribute__((ext_vector_type(4))) float f32x4;
typedef __attribute__((ext_vector_type(2))) float f32x2;
typedef __attribute__((ext_vector_type(4))) int i32x4;
typedef __attribute__((ext_vector_type(4))) unsigned u32x4;
typedef __attribute__((ext_vector_type(2))) unsigned u32x2;
#define LAS __attribute__((address_space(3)))
#define DEVI __device__ __forceinline__

#define PFV 1
constexpr int S_ = 2048, D_ = 1024, F_ = 3584;
constexpr unsigned OOB = 0x80000000u;

DEVI void wait_lgkm0() { asm volatile("s_waitcnt lgkmcnt(0)" ::: "memory"); }
template <int N> struct IC { static constexpr int v = N; };
template <int I, int N, class F> DEVI void sfor(F&& f) { if constexpr (I < N) { f(IC<I>{}); sfor<I + 1, N>(f); } }
#define TLR(n) do {} while (0)
#define TLW(k) do {} while (0)
#define TLS(k, i) do {} while (0)

DEVI i32x4 make_srd(const void* p, unsigned bytes) {
  unsigned long long a = (unsigned long long)p;
  i32x4 r;
  r.x = (int)(unsigned)a; r.y = (int)((unsigned)(a >> 32) & 0xffffu); r.z = (int)bytes; r.w = 0x00020000;
  return r;
}
DEVI void dma16(unsigned lds_addr, unsigned voff, i32x4 srd, unsigned soff) {
  asm volatile("s_mov_b32 m0, %0\n\ts_nop 0\n\tbuffer_load_dwordx4 %1, %2, %3 offen lds"
               :: "s"(lds_addr), "v"(voff), "s"(srd), "s"(soff));
}
DEVI void touch_lines(unsigned& sink, unsigned voff, i32x4 srd, unsigned soff) {
  asm volatile("buffer_load_dword %0, %1, %2, %3 offen" : "+v"(sink) : "v"(voff), "s"(srd), "s"(soff));
}
template <int N> DEVI void wait_vm() { asm volatile("s_waitcnt vmcnt(%0)" :: "n"(N) : "memory"); }
DEVI void bar() { asm volatile("" ::: "memory"); __builtin_amdgcn_s_barrier(); asm volatile("" ::: "memory"); }
#define SB() __builtin_amdgcn_sched_barrier(0)

DEVI bf16x8 lds_b128(const LAS char* p) { return *(const LAS bf16x8*)p; }
DEVI s16x4 lds_tr(const LAS char* p) { return __builtin_amdgcn_ds_read_tr16_b64_v4i16((LAS s16x4*)p); }
DEVI bf16x8 cat8(s16x4 a, s16x4 b) { return __builtin_bit_cast(bf16x8, __builtin_shufflevector(a, b, 0, 1, 2, 3, 4, 5, 6, 7)); }
DEVI bf16x8 cat8u(u32x2 a, u32x2 b) { return __builtin_bit_cast(bf16x8, (u32x4){a[0], a[1], b[0], b[1]}); }
DEVI f32x4 mfma(bf16x8 a, bf16x8 b, f32x4 c) { return __builtin_amdgcn_mfma_f32_16x16x32_bf16(a, b, c, 0, 0, 0); }
DEVI void mfma_ip(f32x4& c, bf16x8 a, bf16x8 b) { asm volatile("v_mfma_f32_16x16x32_bf16 %0, %1, %2, %0" : "+v"(c) : "v"(a), "v"(b)); }
template <class T> DEVI void pin(T& v) { asm volatile("" : "+v"(v)); }
DEVI void zero_pin(f32x4& v) { v = (f32x4){0, 0, 0, 0}; pin(v); }
DEVI void mfma_fence() { asm volatile("s_nop 7\n\ts_nop 7" ::: "memory"); }
DEVI unsigned pk_bf16(float lo, float hi) { bf16x2 v = {(__bf16)lo, (__bf16)hi}; return __builtin_bit_cast(unsigned, v); }
DEVI u32x2 pk4(f32x4 v) { return (u32x2){pk_bf16(v[0], v[1]), pk_bf16(v[2], v[3])}; }
DEVI float bf_lo(unsigned u) { return __uint_as_float(u << 16); }
DEVI float bf_hi(unsigned u) { return __uint_as_float(u & 0xffff0000u); }
DEVI float rbf(float x) { return (float)(__bf16)x; }
DEVI float red16_add(float x) { auto s = __builtin_amdgcn_permlane16_swap(__float_as_uint(x), __float_as_uint(x), false, false); return __uint_as_float(s[0]) + __uint_as_float(s[1]); }
DEVI float red32_add(float x) { auto s = __builtin_amdgcn_permlane32_swap(__float_as_uint(x), __float_as_uint(x), false, false); return __uint_as_float(s[0]) + __uint_as_float(s[1]); }
DEVI float red16_max(float x) { auto s = __builtin_amdgcn_permlane16_swap(__float_as_uint(x), __float_as_uint(x), false, false); return fmaxf(__uint_as_float(s[0]), __uint_as_float(s[1])); }
DEVI float red32_max(float x) { auto s = __builtin_amdgcn_permlane32_swap(__float_as_uint(x), __float_as_uint(x), false, false); return fmaxf(__uint_as_float(s[0]), __uint_as_float(s[1])); }
DEVI float ln_rstd(float var) { return 1.0f / rbf(sqrtf(rbf(rbf(var) + 1e-5f))); }

DEVI float wave_sum(float x) {
  x += __builtin_bit_cast(float, __builtin_amdgcn_update_dpp(0, __builtin_bit_cast(int, x), 0xB1, 0xf, 0xf, true));
  x += __builtin_bit_cast(float, __builtin_amdgcn_update_dpp(0, __builtin_bit_cast(int, x), 0x4E, 0xf, 0xf, true));
  x += __builtin_bit_cast(float, __builtin_amdgcn_update_dpp(0, __builtin_bit_cast(int, x), 0x141, 0xf, 0xf, true));
  x += __builtin_bit_cast(float, __builtin_amdgcn_update_dpp(0, __builtin_bit_cast(int, x), 0x140, 0xf, 0xf, true));
  return red32_add(red16_add(x));
}

__global__ __launch_bounds__(512) void k0_ln(const u16* __restrict__ x, u16* __restrict__ xn, float* __restrict__ xmean,
                                             const u16* __restrict__ wo, u16* __restrict__ wbar, const u16* __restrict__ wqkv) {
  const int lane = threadIdx.x & 63, w = threadIdx.x >> 6, row = blockIdx.x * 8 + w;
  TLS(0, 0);
  const u32x4* src = (const u32x4*)(x + (size_t)row * D_);
  const u32x4 a = src[lane], b = src[lane + 64];
  u32x4 wa, wb;
  const bool dow = __builtin_amdgcn_readfirstlane(w) < 4;
  if (dow) { const u32x4* ws = (const u32x4*)(wo + (size_t)(blockIdx.x * 4 + w) * D_); wa = ws[lane]; wb = ws[lane + 64]; }
  unsigned pfv = 0;
  if (lane < 24) pfv = *(const unsigned*)(wqkv + (size_t)(row * 24 + lane) * 64);
  float v[16];
#pragma unroll
  for (int i = 0; i < 4; ++i) { v[2 * i] = bf_lo(a[i]); v[2 * i + 1] = bf_hi(a[i]); v[8 + 2 * i] = bf_lo(b[i]); v[8 + 2 * i + 1] = bf_hi(b[i]); }
  float s = 0.f;
#pragma unroll
  for (int i = 0; i < 16; ++i) s += v[i];
  const float mean = wave_sum(s) * (1.f / D_);
  float q = 0.f;
#pragma unroll
  for (int i = 0; i < 16; ++i) { const float d = v[i] - mean; q += d * d; }
  const float mb = rbf(mean), rs = ln_rstd(wave_sum(q) * (1.f / D_));
  u32x4 oa, ob;
#pragma unroll
  for (int i = 0; i < 4; ++i) {
    const unsigned da = pk_bf16(v[2 * i] - mb, v[2 * i + 1] - mb), db = pk_bf16(v[8 + 2 * i] - mb, v[8 + 2 * i + 1] - mb);
    oa[i] = pk_bf16(bf_lo(da) * rs, bf_hi(da) * rs);
    ob[i] = pk_bf16(bf_lo(db) * rs, bf_hi(db) * rs);
  }
  u32x4* dst = (u32x4*)(xn + (size_t)row * D_);
  dst[lane] = oa; dst[lane + 64] = ob;
  if (lane == 0) xmean[row] = mean;
  if (dow) {
    float t = 0.f;
#pragma unroll
    for (int i = 0; i < 4; ++i) t += (bf_lo(wa[i]) + bf_hi(wa[i])) + (bf_lo(wb[i]) + bf_hi(wb[i]));
    t = wave_sum(t) * (1.f / D_);
    const float hi = rbf(t);
    if (lane < 2) wbar[lane * D_ + blockIdx.x * 4 + w] = (u16)(__float_as_uint(lane ? rbf(t - hi) : hi) >> 16);
  }
  asm volatile("" :: "v"(pfv));
  TLS(0, 3);
}

constexpr int K1_STAGE = 18 * 1024 + 24 * 1024;
constexpr int K1_NS = 3;
constexpr int K1_DUMMY = K1_STAGE * K1_NS;
constexpr int K1_LDS = K1_DUMMY + 1024;
struct K1Frag { bf16x8 x[4], xh; s16x4 w[3][2][2]; };

__global__ __launch_bounds__(512, 2) void k1_qkv_attn(const u16* __restrict__ xn, const u16* __restrict__ wqkv, u16* __restrict__ o,
                                                      const u16* __restrict__ wg, const u16* __restrict__ wu) {
  __shared__ __attribute__((aligned(16))) char smem_[K1_LDS];
  LAS char* smem = (LAS char*)smem_;
  const unsigned lds0 = (unsigned)(size_t)smem;
  const int tid = threadIdx.x, lane = tid & 63, w = __builtin_amdgcn_readfirstlane(tid >> 6);
  const int bid = blockIdx.x, xcd = bid & 7, idx = bid >> 3;
  const int tm = 4 * (xcd >> 1) + (idx & 3), hp = 8 * (xcd & 1) + (idx >> 2);
  const int r0 = tm * 128;
  constexpr int NT = D_ / 64;
  TLS(1, 0);
  if (w >= 4) {
    const int lw = w - 4;
    const i32x4 srdA = make_srd(xn, S_ * D_ * 2), srdW = make_srd(wqkv, D_ * 3 * D_ * 2);
    const int lr = lane >> 3, lc = lane & 7;
    const unsigned voffA = (unsigned)((8 * lw + lr) * (D_ * 2) + ((lc ^ ((4 * lw + (lane >> 4)) & 7)) << 4));
    const int fW = ((lane >> 4) & 1) | ((lw & 1) << 1);
    const unsigned voffW = (unsigned)((8 * lw + lr) * (3 * D_ * 2) + (((((lc >> 1) ^ fW) << 1) | (lc & 1)) << 4));
    const unsigned sA = (unsigned)r0 * (D_ * 2), sW = (unsigned)hp * 128;
    const bool halo = lw < 2;
    const i32x4 srdX = halo ? srdA : make_srd(lw == 2 ? wg : wu, D_ * F_ * 2);
    const unsigned voffX = halo ? voffA : OOB;
    const unsigned sX = halo ? (unsigned)(r0 >= 16 ? r0 - 16 : 0) * (D_ * 2) : (unsigned)bid * 28672u, dX = halo ? 128u : 1792u;
    auto issue = [&](int t, int slot) {
      const unsigned lb = lds0 + slot * K1_STAGE + lw * 1024;
#pragma unroll
      for (int j = 0; j < 2; ++j) dma16(lb + 18432 + j * 4096, voffW, srdW, sW + (unsigned)(t * 64 + 32 * j) * (3 * D_ * 2));
#pragma unroll
      for (int j = 0; j < 4; ++j) dma16(lb + j * 4096, voffA, srdA, sA + (unsigned)(32 * j) * (D_ * 2) + t * 128);
#pragma unroll
      for (int s = 1; s < 3; ++s)
#pragma unroll
        for (int j = 0; j < 2; ++j) dma16(lb + 18432 + s * 8192 + j * 4096, voffW, srdW, sW + (unsigned)(t * 64 + 32 * j) * (3 * D_ * 2) + s * (D_ * 2));
      dma16(halo ? lb + 16384 : lds0 + K1_DUMMY, voffX, srdX, sX + t * dX);
    };
    TLR(a);
    issue(0, 0);
    TLR(b);
    issue(1, 1);
    TLR(c);
    wait_vm<11>();
    TLR(d);
    bar();
    issue(2, 2);
    int slot = 0;
    for (int t = 0; t < NT - 2; ++t) {
      wait_vm<11>(); bar();
      if (t + 3 < NT) issue(t + 3, slot);
      slot = slot == 2 ? 0 : slot + 1;
    }
    wait_vm<0>(); bar();
    {
      const i32x4 srdP = make_srd((lw & 1) ? wu : wg, D_ * F_ * 2);
      const unsigned o_ = (unsigned)bid * 28672u + (lw >> 1) * 14336u;
      dma16(lds0 + K1_DUMMY, (unsigned)lane * 128u, srdP, o_);
      dma16(lds0 + K1_DUMMY, lane < 48 ? (unsigned)lane * 128u : OOB, srdP, o_ + 8192u);
    }
    TLW(1);
    return;
  }
  const int hh = w & 1, rh = w >> 1, g = lane >> 4, c = lane & 15;
  const int sw = (lane >> 1) & 7;
  const int aoff0 = (64 * rh + c) * 128 + ((g ^ sw) << 4), hoff0 = (128 + c) * 128 + ((g ^ sw) << 4);
  const int q4 = c >> 2, p4 = lane & 3, fR = ((q4 >> 1) & 1) | ((g & 1) << 1);
  const int woff0 = 18432 + (8 * g + q4) * 128 + (((2 * hh + rh) ^ fR) << 5) + 8 * p4;
  auto rd = [&](K1Frag& f, int slot, int ks, auto r_c) {
    constexpr int r = decltype(r_c)::v;
    const LAS char* b = smem + slot * K1_STAGE;
    if constexpr (r >= 2 && r <= 5) f.x[r - 2] = lds_b128(b + (aoff0 ^ (ks << 6)) + (r - 2) * 2048);
    else if constexpr (r == 12) f.xh = lds_b128(b + (hoff0 ^ (ks << 6)));
    else {
      constexpr int rr = r < 2 ? r : (r < 12 ? r - 4 : r - 5);
      constexpr int mat = rr >> 2, sl = (rr >> 1) & 1, hf = rr & 1;
      f.w[mat][sl][hf] = lds_tr(b + (woff0 ^ (sl << 5)) + ks * 4096 + mat * 8192 + hf * 512);
    }
  };
  f32x4 aq[4][2], ak[4][2], av[4][2], ahk, ahv;
#pragma unroll
  for (int m = 0; m < 4; ++m)
#pragma unroll
    for (int s = 0; s < 2; ++s) { zero_pin(aq[m][s]); zero_pin(ak[m][s]); zero_pin(av[m][s]); }
  zero_pin(ahk); zero_pin(ahv);
  constexpr int NM = 26;
  auto mm = [&](const K1Frag& f, auto i_c) {
    constexpr int i = decltype(i_c)::v;
    if constexpr (i < 8) mfma_ip(aq[i & 3][i >> 2], cat8(f.w[0][i >> 2][0], f.w[0][i >> 2][1]), f.x[i & 3]);
    else if constexpr (i < 16) mfma_ip(ak[i & 3][(i - 8) >> 2], cat8(f.w[1][(i - 8) >> 2][0], f.w[1][(i - 8) >> 2][1]), f.x[i & 3]);
    else if constexpr (i == 16) mfma_ip(ahk, cat8(f.w[1][0][0], f.w[1][0][1]), f.xh);
    else if constexpr (i < 25) mfma_ip(av[(i - 17) & 3][(i - 17) >> 2], f.x[(i - 17) & 3], cat8(f.w[2][(i - 17) >> 2][0], f.w[2][(i - 17) >> 2][1]));
    else mfma_ip(ahv, f.xh, cat8(f.w[2][0][0], f.w[2][0][1]));
  };
  K1Frag fa, fb;
  __builtin_amdgcn_s_setprio(1);
  bar();
  TLS(1, 1);
  sfor<0, 17>([&](auto r) { rd(fa, 0, 0, r); });
  mfma_fence(); SB();
  constexpr int NB = 4;
  sfor<0, NT>([&](auto t_c) {
    constexpr int t = decltype(t_c)::v, slot = t % K1_NS;
    constexpr bool last = t == NT - 1;
    sfor<0, NM>([&](auto i) { mm(fa, i); if constexpr (decltype(i)::v < 17) rd(fb, slot, 1, i); SB(); });
    sfor<0, NB>([&](auto i) { mm(fb, i); SB(); });
    if constexpr (!last) { wait_lgkm0(); bar(); SB(); }
    sfor<NB, NM>([&](auto i) {
      mm(fb, i);
      if constexpr (!last && decltype(i)::v - NB < 17) rd(fa, (slot + 1) % K1_NS, 0, IC<decltype(i)::v - NB>{});
      SB();
    });
  });
  mfma_fence(); SB();
  TLS(1, 2);
  LAS char* ex = smem + K1_STAGE;
  auto exa = [&](int h_, int r_, int kind, int b) { return (LAS u32x2*)(ex + ((((h_ * 2 + r_) * 2 + kind) * 2 + b) * 64 + lane) * 8); };
  u32x2 qp[4][2], kp[4][2], vp[4][2];
#pragma unroll
  for (int s = 0; s < 2; ++s) { kp[3][s] = pk4(ak[3][s]); vp[3][s] = pk4(av[3][s]); }
  *exa(hh, 0, 0, rh) = pk4(ahk); *exa(hh, 0, 1, rh) = pk4(ahv);
  if (rh == 0) {
    *exa(hh, 1, 0, 0) = kp[3][0]; *exa(hh, 1, 0, 1) = kp[3][1];
    *exa(hh, 1, 1, 0) = vp[3][0]; *exa(hh, 1, 1, 1) = vp[3][1];
  }
#pragma unroll
  for (int m = 0; m < 4; ++m)
#pragma unroll
    for (int s = 0; s < 2; ++s) { qp[m][s] = pk4(aq[m][s]); if (m < 3) { kp[m][s] = pk4(ak[m][s]); vp[m][s] = pk4(av[m][s]); } }
  wait_lgkm0(); bar();
  u32x2 kprev[2], vprev[2];
#pragma unroll
  for (int s = 0; s < 2; ++s) { kprev[s] = *exa(hh, rh, 0, s ^ rh); vprev[s] = *exa(hh, rh, 1, s ^ rh); }
  const bool odd = c & 1;
  const int d1 = c - 4 * g - (c & 1);
  const float NINF = -__builtin_inff();
  const float b0a = d1 + 16 <= 14 ? 0.f : NINF, b0b = d1 + 14 <= 14 ? 0.f : NINF, b1a = d1 >= 0 ? 0.f : NINF, b1b = d1 - 2 >= 0 ? 0.f : NINF;
  const bool first = (r0 + 64 * rh) == 0;
  const unsigned pmask = odd ? 0xffff0000u : 0x0000ffffu;
  const bf16x8 ones = __builtin_bit_cast(bf16x8, (u32x4){0x3f803f80u, 0x3f803f80u, 0x3f803f80u, 0x3f803f80u});
  const float SL2E = 0.17677669529663687f * 1.4426950408889634f;
  const int dim_off = (((g & 1) ^ rh) << 4) + ((g >> 1) << 3);
  u16* orow = o + (size_t)(r0 + 64 * rh + c) * D_ + (hp * 2 + hh) * 32 + dim_off;
#pragma unroll
  for (int mi = 0; mi < 4; ++mi) {
    const int m = (mi + 1) & 3;
    const bf16x8 qf = cat8u(qp[m][0], qp[m][1]);
    const f32x4 s0 = mfma(m ? cat8u(kp[m - 1][0], kp[m - 1][1]) : cat8u(kprev[0], kprev[1]), qf, (f32x4){0, 0, 0, 0});
    const f32x4 s1 = mfma(cat8u(kp[m][0], kp[m][1]), qf, (f32x4){0, 0, 0, 0});
    const float l0 = fmaf(odd ? s0[1] : s0[0], SL2E, (m == 0 && first) ? NINF : b0a);
    const float l1 = fmaf(odd ? s0[3] : s0[2], SL2E, (m == 0 && first) ? NINF : b0b);
    const float l2 = fmaf(odd ? s1[1] : s1[0], SL2E, b1a);
    const float l3 = fmaf(odd ? s1[3] : s1[2], SL2E, b1b);
    const float mx = red32_max(red16_max(fmaxf(fmaxf(l0, l1), fmaxf(l2, l3))));
    const float p0 = __builtin_amdgcn_exp2f(l0 - mx), p1 = __builtin_amdgcn_exp2f(l1 - mx), p2 = __builtin_amdgcn_exp2f(l2 - mx), p3 = __builtin_amdgcn_exp2f(l3 - mx);
    const bf16x8 pf = __builtin_bit_cast(bf16x8, (u32x4){pk_bf16(p0, p0) & pmask, pk_bf16(p1, p1) & pmask, pk_bf16(p2, p2) & pmask, pk_bf16(p3, p3) & pmask});
    const f32x4 sum = mfma(ones, pf, (f32x4){0, 0, 0, 0});
    const f32x4 o0 = mfma(cat8u(m ? vp[m - 1][0] : vprev[0], vp[m][0]), pf, (f32x4){0, 0, 0, 0});
    const f32x4 o1 = mfma(cat8u(m ? vp[m - 1][1] : vprev[1], vp[m][1]), pf, (f32x4){0, 0, 0, 0});
    const float inv = __builtin_amdgcn_rcpf(sum[0]);
    const u32x2 op0 = pk4(o0 * inv), op1 = pk4(o1 * inv);
    auto e0 = __builtin_amdgcn_permlane16_swap(op0[0], op1[0], false, false);
    auto e1 = __builtin_amdgcn_permlane16_swap(op0[1], op1[1], false, false);
    *(u32x4*)(orow + (size_t)(16 * m) * D_) = (u32x4){e0[0], e1[0], e0[1], e1[1]};
  }
  TLS(1, 3);
}

constexpr int G64_STAGE = 24 * 1024, G64_NS = 4, G64_WB = G64_STAGE * G64_NS;
struct G64Frag { bf16x8 x[2][2], wb[2]; s16x4 w[4][2][2]; };

template <int K, bool LN2>
__global__ __launch_bounds__(512, 2) void k_gemm_n1024(const u16* __restrict__ A, const u16* __restrict__ W, const u16* __restrict__ R,
                                                        u16* __restrict__ O, u16* __restrict__ OC, float* __restrict__ ssq,
                                                        const float* __restrict__ xmean, const u16* __restrict__ wbar) {
  __shared__ __attribute__((aligned(16))) char smem_[G64_WB + (LN2 ? 4096 : 0)];
  LAS char* smem = (LAS char*)smem_;
  const unsigned lds0 = (unsigned)(size_t)smem;
  const int tid = threadIdx.x, lane = tid & 63, w = __builtin_amdgcn_readfirstlane(tid >> 6);
  const int bid = blockIdx.x, xcd = bid & 7, idx = bid >> 3;
  const int tm = 4 * (xcd >> 1) + (idx & 3), tn = 8 * (xcd & 1) + (idx >> 2);
  const int r0 = tm * 128, c0 = tn * 64;
  constexpr int NT = K / 64;
  TLS(LN2 ? 2 : 4, 0);
  if (w >= 4) {
    const int lw = w - 4;
    const i32x4 srdA = make_srd(A, S_ * K * 2), srdW = make_srd(W, K * D_ * 2);
    const int lr = lane >> 3, lc = lane & 7;
    const unsigned voffA = (unsigned)((8 * lw + lr) * (K * 2) + ((lc ^ ((4 * lw + (lane >> 4)) & 7)) << 4));
    const int fW = ((lane >> 4) & 1) | ((lw & 1) << 1);
    const unsigned voffW = (unsigned)((8 * lw + lr) * (D_ * 2) + (((((lc >> 1) ^ fW) << 1) | (lc & 1)) << 4));
    const unsigned sA = (unsigned)r0 * (K * 2), sW = (unsigned)c0 * 2;
    auto issue = [&](int t, int slot) {
      const unsigned lb = lds0 + slot * G64_STAGE + lw * 1024;
#pragma unroll
      for (int j = 0; j < 2; ++j) dma16(lb + 16384 + j * 4096, voffW, srdW, sW + (unsigned)(t * 64 + 32 * j) * (D_ * 2));
#pragma unroll
      for (int j = 0; j < 4; ++j) dma16(lb + j * 4096, voffA, srdA, sA + (unsigned)(32 * j) * (K * 2) + t * 128);
    };
    if constexpr (LN2) dma16(lds0 + G64_WB + lw * 1024, (unsigned)lane * 16u, make_srd(wbar, 4096), (unsigned)lw * 1024u);
    issue(0, 0); issue(1, 1);
    wait_vm<6>(); bar();
    issue(2, 2); issue(3, 3);
    for (int t = 0; t < NT - G64_NS; ++t) { wait_vm<12>(); bar(); issue(t + G64_NS, t & 3); }
    wait_vm<12>(); bar();
    wait_vm<6>(); bar();
    wait_vm<0>(); bar();
    return;
  }
  const int g = lane >> 4, c = lane & 15;
  const int ecol = c0 + ((g & 1) << 4) + ((g >> 1) << 3);
  u32x4 res[2][2];
  float xm[2];
#pragma unroll
  for (int m = 0; m < 2; ++m) {
#pragma unroll
    for (int jp = 0; jp < 2; ++jp) res[m][jp] = *(const u32x4*)(R + (size_t)(r0 + 32 * w + 16 * m + c) * D_ + ecol + 32 * jp);
    if constexpr (LN2) xm[m] = xmean[r0 + 32 * w + 16 * m + c];
  }
  const int sw = (lane >> 1) & 7;
  const int aoff0 = (32 * w + c) * 128 + ((g ^ sw) << 4);
  const int q4 = c >> 2, p4 = lane & 3, fR = ((q4 >> 1) & 1) | ((g & 1) << 1);
  const int woff0 = 16384 + (8 * g + q4) * 128 + (fR << 5) + 8 * p4;
  const int wboff = G64_WB + (c & 1) * 2048 + g * 16;
  constexpr int NR = LN2 ? 22 : 20;
  auto rd = [&](G64Frag& f, int t, auto slot_c, auto r_c) {
    constexpr int r = decltype(r_c)::v, ks = r / 10, q = r % 10;
    const LAS char* b = smem + decltype(slot_c)::v * G64_STAGE;
    if constexpr (r >= 20) f.wb[r - 20] = lds_b128(smem + wboff + t * 128 + (r - 20) * 64);
    else if constexpr (q < 2) f.x[q][ks] = lds_b128(b + (aoff0 ^ (ks << 6)) + q * 2048);
    else f.w[(q - 2) >> 1][ks][q & 1] = lds_tr(b + (woff0 ^ (((q - 2) >> 1) << 5)) + ks * 4096 + (q & 1) * 512);
  };
  f32x4 acc[2][4], accm[2];
#pragma unroll
  for (int m = 0; m < 2; ++m) {
    if constexpr (LN2) zero_pin(accm[m]);
#pragma unroll
    for (int j = 0; j < 4; ++j) zero_pin(acc[m][j]);
  }
  constexpr int NM = LN2 ? 20 : 16;
  auto mm = [&](const G64Frag& f, auto i_c) {
    constexpr int i = decltype(i_c)::v, ks = i >> 3, j = (i >> 1) & 3, m = i & 1;
    if constexpr (i < 16) mfma_ip(acc[m][j], cat8(f.w[j][ks][0], f.w[j][ks][1]), f.x[m][ks]);
    else mfma_ip(accm[m], f.wb[j & 1], f.x[m][j & 1]);
  };
  G64Frag fr[2];
  bar();
  TLS(LN2 ? 2 : 4, 1);
  sfor<0, NR>([&](auto r) { rd(fr[0], 0, IC<0>{}, r); });
  mfma_fence(); SB();
  auto step = [&](int t, auto slot_c, auto last_c) {
    constexpr int slot = decltype(slot_c)::v;
    if constexpr (!decltype(last_c)::v) { wait_lgkm0(); bar(); SB(); }
    sfor<0, NM>([&](auto i) {
      constexpr int ii = decltype(i)::v;
      if constexpr (!decltype(last_c)::v && 2 * ii < NR) {
        rd(fr[(slot + 1) & 1], t + 1, IC<(slot + 1) % G64_NS>{}, IC<2 * ii>{}); rd(fr[(slot + 1) & 1], t + 1, IC<(slot + 1) % G64_NS>{}, IC<2 * ii + 1>{});
      }
      mm(fr[slot & 1], i);
      SB();
    });
  };
  sfor<0, NT>([&](auto t_c) { constexpr int t = decltype(t_c)::v; step(t, IC<t % G64_NS>{}, IC<(t == NT - 1) ? 1 : 0>{}); });
  mfma_fence(); SB();
  TLS(LN2 ? 2 : 4, 2);
#pragma unroll
  for (int m = 0; m < 2; ++m) {
    const int row = r0 + 32 * w + 16 * m + c;
    float mean = 0.f, mb = 0.f, q = 0.f;
    if constexpr (LN2) { mean = xm[m] + (accm[m][0] + accm[m][1]); mb = rbf(mean); }
#pragma unroll
    for (int jp = 0; jp < 2; ++jp) {
      const u32x2 a0 = pk4(acc[m][2 * jp]), a1 = pk4(acc[m][2 * jp + 1]);
      auto e0 = __builtin_amdgcn_permlane16_swap(a0[0], a1[0], false, false);
      auto e1 = __builtin_amdgcn_permlane16_swap(a0[1], a1[1], false, false);
      const unsigned e[4] = {e0[0], e1[0], e0[1], e1[1]};
      u32x4 ov, oc;
#pragma unroll
      for (int i = 0; i < 4; ++i) {
        ov[i] = pk_bf16(bf_lo(e[i]) + bf_lo(res[m][jp][i]), bf_hi(e[i]) + bf_hi(res[m][jp][i]));
        if constexpr (LN2) {
          const float h0 = bf_lo(ov[i]), h1 = bf_hi(ov[i]);
          oc[i] = pk_bf16(h0 - mb, h1 - mb);
          q += (h0 - mean) * (h0 - mean) + (h1 - mean) * (h1 - mean);
        }
      }
      *(u32x4*)(O + (size_t)row * D_ + ecol + 32 * jp) = ov;
      if constexpr (LN2) *(u32x4*)(OC + (size_t)row * D_ + ecol + 32 * jp) = oc;
    }
    if constexpr (LN2) {
      q = red32_add(red16_add(q));
      if (g == 0) ssq[row * 16 + tn] = q;
    }
  }
  TLS(LN2 ? 2 : 4, 3);
}

constexpr int K3_AH = 16384;
constexpr int K3_UOFF = 15360 + 16;
constexpr int K3_BB = K3_UOFF + 15360;
constexpr int K3_B0 = 4 * K3_AH;
constexpr int K3_TAB = K3_B0 + 3 * K3_BB;
constexpr int K3_DUMMY = K3_TAB + 1024;
constexpr int K3_LDS = K3_DUMMY + 1024;

__global__ __launch_bounds__(512, 2) void k3_geglu(const u16* __restrict__ hc, const u16* __restrict__ wg, const u16* __restrict__ wu,
                                                    const float* __restrict__ ssq, u16* __restrict__ act, const u16* __restrict__ wpf) {
  __shared__ __attribute__((aligned(16))) char smem_[K3_LDS];
  LAS char* smem = (LAS char*)smem_;
  const unsigned lds0 = (unsigned)(size_t)smem;
  const int tid = threadIdx.x, lane = tid & 63, w = __builtin_amdgcn_readfirstlane(tid >> 6);
  const int grp = w >> 2, w4 = w & 3, wm = (w >> 1) & 1, wn = w & 1, g = lane >> 4, c = lane & 15;
  const int bid = blockIdx.x, xcd = bid & 7, idx = bid >> 3;
  const int tm = 4 * (xcd & 1) + (idx & 3), tn = 8 * (xcd >> 1) + (idx >> 2);
  const int r0 = tm * 256, f0 = tn * 112;
  TLS(3, 0);
  const i32x4 srdA = make_srd(hc, S_ * D_ * 2), srdB = make_srd(grp ? wu : wg, D_ * F_ * 2);
  const bool pfw = w4 == 3;
  const i32x4 srdB3 = make_srd(pfw ? wpf : (grp ? wu : wg), D_ * F_ * 2);
  const int lr = lane >> 3, lc = lane & 7;
  const unsigned voffA = (unsigned)((8 * w4 + lr) * (D_ * 2) + ((lc ^ ((4 * w4 + (lane >> 4)) & 7)) << 4));
  unsigned voffB[4];
#pragma unroll
  for (int j = 0; j < 4; ++j) {
    const int ch = (w4 + 4 * j) * 64 + lane, gr = ch / 120, r = ch - gr * 120, rr = r / 14, pos = r - rr * 14;
    voffB[j] = r < 112 ? (unsigned)((8 * gr + rr) * (F_ * 2) + (pos << 4)) : OOB;
  }
  if (pfw) voffB[3] = lane < 8 ? (unsigned)lane * 128u : OOB;
  const unsigned sA = (unsigned)r0 * (D_ * 2), sB = (unsigned)f0 * 2;
  auto dmaA = [&](int t, int half, int buf, int j) {
    dma16(lds0 + (half * 2 + buf) * K3_AH + w4 * 1024 + j * 4096, voffA, srdA, sA + (unsigned)(128 * half + 32 * j) * (D_ * 2) + t * 128);
  };
  auto dmaB = [&](int t, int buf, int j) {
    const unsigned lb = lds0 + K3_B0 + buf * K3_BB + grp * K3_UOFF + w4 * 1024 + j * 4096, so = sB + (unsigned)(t * 64) * (F_ * 2);
    if (j < 3) dma16(lb, voffB[j], srdB, so);
    else dma16(pfw ? lds0 + K3_DUMMY : lb, voffB[3], srdB3, pfw ? (unsigned)bid * 28672u + (unsigned)(2 * (t - 2) + grp) * 1024u : so);
  };
  const int sw = (lane >> 1) & 7;
  const int aoff0 = grp * 2 * K3_AH + (64 * wm + c) * 128 + ((g ^ sw) << 4);
  const int q4 = c >> 2, p4 = lane & 3;
  const int boffP = K3_B0 + g * 1920 + q4 * 224 + wn * 112 + 8 * p4;
  const int boffM = K3_B0 + g * 1920 + q4 * 224 + wn * 112 + 96 + (p4 >> 1) * K3_UOFF + 8 * (p4 & 1);
  bf16x8 fx[4][2]; s16x4 fw[7][2][2];
  auto rd = [&](int abuf, int bbuf, auto r_c) {
    constexpr int r = decltype(r_c)::v, ks = r / 18, q = r % 18;
    if constexpr (q < 14) {
      constexpr int j = q >> 1, hf = q & 1;
      fw[j][ks][hf] = lds_tr(smem + (j == 6 ? boffM : boffP) + bbuf * K3_BB + (j < 3 ? j * 32 : (j < 6 ? K3_UOFF + (j - 3) * 32 : 0)) + ks * 7680 + hf * 896);
    } else fx[q - 14][ks] = lds_b128(smem + (aoff0 ^ (ks << 6)) + abuf * K3_AH + (q - 14) * 2048);
  };
  f32x4 acc[4][7];
#pragma unroll
  for (int m = 0; m < 4; ++m)
#pragma unroll
    for (int j = 0; j < 7; ++j) zero_pin(acc[m][j]);
  auto mma = [&]() {
    __builtin_amdgcn_s_setprio(1);
#pragma unroll
    for (int ks = 0; ks < 2; ++ks)
#pragma unroll
      for (int j = 0; j < 7; ++j)
#pragma unroll
        for (int m = 0; m < 4; ++m) mfma_ip(acc[m][j], cat8(fw[j][ks][0], fw[j][ks][1]), fx[m][ks]);
    __builtin_amdgcn_s_setprio(0);
  };
  constexpr int NT = D_ / 64;
  if (grp == 0) {
#pragma unroll
    for (int j = 0; j < 4; ++j) { dmaA(0, 0, 0, j); dmaB(0, 0, j); }
    wait_vm<0>(); bar();
#pragma unroll
    for (int j = 0; j < 4; ++j) dmaB(1, 1, j);
  } else {
#pragma unroll
    for (int j = 0; j < 4; ++j) dmaB(0, 0, j);
#pragma unroll
    for (int j = 0; j < 4; ++j) dmaA(0, 1, 0, j);
    wait_vm<4>(); bar();
    f32x4 st[4];
    const float* sp = ssq + (size_t)(r0 + (tid & 255)) * 16;
#pragma unroll
    for (int i = 0; i < 4; ++i) asm volatile("global_load_dwordx4 %0, %1, off offset:%2" : "=v"(st[i]) : "v"(sp), "n"(i * 16));
#pragma unroll
    for (int j = 0; j < 4; ++j) { dmaA(1, 0, 1, j); dmaB(1, 1, j); }
    asm volatile("s_waitcnt vmcnt(8)" : "+v"(st[0]), "+v"(st[1]), "+v"(st[2]), "+v"(st[3]) :: "memory");
    const f32x4 q = (st[0] + st[1]) + (st[2] + st[3]);
    *(LAS float*)(smem + K3_TAB + (tid & 255) * 4) = ln_rstd(((q[0] + q[1]) + (q[2] + q[3])) * (1.f / D_));
    wait_lgkm0(); bar();
  }
  mfma_fence(); SB();
  const int ahalf = grp ^ 1;
  sfor<0, NT>([&](auto t_c) {
    constexpr int t = decltype(t_c)::v, ab = t & 1, bb = t % 3;
    const int abuf = ab ^ ahalf;
    sfor<0, 36>([&](auto r) {
      constexpr int rr = decltype(r)::v;
      rd(ab, bb, r);
      if constexpr (t < NT - 2 && (rr & 3) == 3 && rr < 32) {
        constexpr int d = rr >> 2;
        if constexpr (d & 1) dmaB(t + 2, (bb + 2) % 3, d >> 1); else dmaA(t + 1 + grp, ahalf, abuf, d >> 1);
      }
      if constexpr (t == NT - 2 && (rr & 7) == 3 && rr < 32) { if (grp == 0) dmaA(t + 1, 1, ab ^ 1, rr >> 3); }
      if constexpr (t < NT - 2) SB();
    });
    wait_lgkm0();
    if constexpr (t == 0) { if (grp == 1) wait_vm<8>(); }
    else if constexpr (t < NT - 2) wait_vm<8>();
    else if constexpr (t == NT - 2) { if (grp == 0) wait_vm<4>(); else wait_vm<0>(); }
    else wait_vm<0>();
    if constexpr (t < NT - 1) bar(); else { if (grp == 0) bar(); }
    SB();
    mma();
    SB();
    if constexpr (t == 0) { if (grp == 0) wait_vm<8>(); }
    if constexpr (t < NT - 1) bar();
    SB();
  });
  mfma_fence(); SB();
  TLS(3, 2);
  const float C0 = -2.0f * 1.4426950408889634f * 0.7978845608028654f, C1 = C0 * 0.044715f;
  auto geglu2 = [&](f32x2 gt, f32x2 up, float c0a, float c1a, float ia2) {
    const f32x2 z = gt * (gt * gt * c1a + c0a);
    f32x2 e; e.x = __builtin_amdgcn_exp2f(z.x); e.y = __builtin_amdgcn_exp2f(z.y);
    const f32x2 d = e * ia2 + ia2;
    f32x2 r; r.x = __builtin_amdgcn_rcpf(d.x); r.y = __builtin_amdgcn_rcpf(d.y);
    const f32x2 o2 = gt * up * r;
    return pk_bf16(o2.x, o2.y);
  };
  const int rowb = 128 * grp + 64 * wm;
  u16* actw = act + (size_t)(r0 + rowb + c) * F_ + f0 + wn * 56;
#pragma unroll
  for (int m = 0; m < 4; ++m) {
    const float a = *(const LAS float*)(smem + K3_TAB + (rowb + 16 * m + c) * 4);
    const float c0a = C0 * a, c1a = C1 * a * a * a, ia2 = __builtin_amdgcn_rcpf(a * a);
    u32x2 pr[3];
#pragma unroll
    for (int b = 0; b < 3; ++b) {
      const f32x4 gt = acc[m][b], up = acc[m][3 + b];
      pr[b] = (u32x2){geglu2((f32x2){gt[0], gt[1]}, (f32x2){up[0], up[1]}, c0a, c1a, ia2), geglu2((f32x2){gt[2], gt[3]}, (f32x2){up[2], up[3]}, c0a, c1a, ia2)};
    }
    auto e0 = __builtin_amdgcn_permlane16_swap(pr[0][0], pr[1][0], false, false);
    auto e1 = __builtin_amdgcn_permlane16_swap(pr[0][1], pr[1][1], false, false);
    *(u32x4*)(actw + (size_t)(16 * m) * F_ + ((g & 1) << 4) + ((g >> 1) << 3)) = (u32x4){e0[0], e1[0], e0[1], e1[1]};
    *(u32x2*)(actw + (size_t)(16 * m) * F_ + 32 + 4 * g) = pr[2];
  }
#pragma unroll
  for (int mp = 0; mp < 2; ++mp) {
    const int mrow = 2 * mp + (g >> 1);
    const float a = *(const LAS float*)(smem + K3_TAB + (rowb + 16 * mrow + c) * 4);
    const float c0a = C0 * a, c1a = C1 * a * a * a, ia2 = __builtin_amdgcn_rcpf(a * a);
    float gt[4], up[4];
#pragma unroll
    for (int r = 0; r < 4; ++r) {
      auto s = __builtin_amdgcn_permlane32_swap(__float_as_uint(acc[2 * mp][6][r]), __float_as_uint(acc[2 * mp + 1][6][r]), false, false);
      gt[r] = __uint_as_float(s[0]); up[r] = __uint_as_float(s[1]);
    }
    const u32x2 pr = {geglu2((f32x2){gt[0], gt[1]}, (f32x2){up[0], up[1]}, c0a, c1a, ia2), geglu2((f32x2){gt[2], gt[3]}, (f32x2){up[2], up[3]}, c0a, c1a, ia2)};
    *(u32x2*)(actw + (size_t)(16 * mrow) * F_ + 48 + 4 * (g & 1)) = pr;
  }
  TLS(3, 3);
}

extern "C" void kernel_launch(void* const* d_in, const int* in_sizes, int n_in, void* d_out, int out_size, void* d_ws, size_t ws_size,
                              hipStream_t stream) {
  const u16* x = (const u16*)d_in[0];
  const u16* wqkv = (const u16*)d_in[1];
  const u16* wo = (const u16*)d_in[2];
  const u16* wg = (const u16*)d_in[4];
  const u16* wu = (const u16*)d_in[5];
  const u16* wd = (const u16*)d_in[6];
  u16* out = (u16*)d_out;
  char* ws = (char*)d_ws;
  u16* hc = (u16*)(ws);
  u16* o = (u16*)(ws + (4u << 20));
  u16* h = (u16*)(ws + (8u << 20));
  float* ssq = (float*)(ws + (12u << 20));
  float* xmean = (float*)(ws + (13u << 20));
  u16* wbar = (u16*)(ws + (14u << 20));
  u16* act = (u16*)(ws + (16u << 20));
  u16* xn = (u16*)(ws + (32u << 20));
  k0_ln<<<256, 512, 0, stream>>>(x, xn, xmean, wo, wbar, wqkv);
  k1_qkv_attn<<<256, 512, 0, stream>>>(xn, wqkv, o, wg, wu);
  k_gemm_n1024<D_, true><<<256, 512, 0, stream>>>(o, wo, x, h, hc, ssq, xmean, wbar);
  k3_geglu<<<256, 512, 0, stream>>>(hc, wg, wu, ssq, act, wd);
  k_gemm_n1024<F_, false><<<256, 512, 0, stream>>>(act, wd, h, out, nullptr, nullptr, nullptr, nullptr);
}
```

```cpp
#include <hip/hip_runtime.h>
#include <hip/hip_bf16.h>

typedef unsigned short u16;
typedef __attribute__((ext_vector_type(8))) __bf16 bf16x8;
typedef __attribute__((ext_vector_type(2))) __bf16 bf16x2;
typedef __attribute__((ext_vector_type(4))) short s16x4;
typedef __attribute__((ext_vector_type(4))) float f32x4;
typedef __attribute__((ext_vector_type(2))) float f32x2;
typedef __attribute__((ext_vector_type(4))) int i32x4;
typedef __attribute__((ext_vector_type(4))) unsigned u32x4;
typedef __attribute__((ext_vector_type(2))) unsigned u32x2;
#define LAS __attribute__((address_space(3)))
#define DEVI __device__ __forceinline__

constexpr int S_ = 2048, D_ = 1024, F_ = 3584;
constexpr size_t WS_HC = 0, WS_O = 4u << 20, WS_H = 8u << 20, WS_SSQ = 12u << 20, WS_XMEAN = 13u << 20, WS_WBAR = 14u << 20, WS_CNT = 15u << 20,
                 WS_ACT = 16u << 20, WS_XN = 32u << 20, WS_PART = 40u << 20;
constexpr unsigned KT_ = S_ * 128;
constexpr unsigned OOB = 0x80000000u;

DEVI void wait_lgkm0() { asm volatile("s_waitcnt lgkmcnt(0)" ::: "memory"); }
template <int N> struct IC { static constexpr int v = N; };
template <int I, int N, class F> DEVI void sfor(F&& f) { if constexpr (I < N) { f(IC<I>{}); sfor<I + 1, N>(f); } }
#define TLR(n) do {} while (0)
#define TLW(k) do {} while (0)
#define TLS(k, i) do {} while (0)

DEVI i32x4 make_srd(const void* p, unsigned bytes) {
  unsigned long long a = (unsigned long long)p;
  i32x4 r;
  r.x = (int)(unsigned)a; r.y = (int)((unsigned)(a >> 32) & 0xffffu); r.z = (int)bytes; r.w = 0x00020000;
  return r;
}
DEVI void dma16(unsigned lds_addr, unsigned voff, i32x4 srd, unsigned soff) {
  asm volatile("s_mov_b32 m0, %0\n\ts_nop 0\n\tbuffer_load_dwordx4 %1, %2, %3 offen lds"
               :: "s"(lds_addr), "v"(voff), "s"(srd), "s"(soff));
}
DEVI void touch_lines(unsigned& sink, unsigned voff, i32x4 srd, unsigned soff) {
  asm volatile("buffer_load_dword %0, %1, %2, %3 offen" : "+v"(sink) : "v"(voff), "s"(srd), "s"(soff));
}
DEVI void dma16_dev(unsigned lds_addr, unsigned voff, i32x4 srd, unsigned soff) {
  asm volatile("s_mov_b32 m0, %0\n\ts_nop 0\n\tbuffer_load_dwordx4 %1, %2, %3 offen sc1 lds"
               :: "s"(lds_addr), "v"(voff), "s"(srd), "s"(soff));
}
template <int N> DEVI void wait_vm() { asm volatile("s_waitcnt vmcnt(%0)" :: "n"(N) : "memory"); }
DEVI void bar() { asm volatile("" ::: "memory"); __builtin_amdgcn_s_barrier(); asm volatile("" ::: "memory"); }
#define SB() __builtin_amdgcn_sched_barrier(0)

DEVI void st_wt(void* p, u32x4 v) { asm volatile("global_store_dwordx4 %0, %1, off sc1\n\ts_nop 1" :: "v"(p), "v"(v) : "memory"); }
DEVI void st_wt(void* p, f32x4 v) { asm volatile("global_store_dwordx4 %0, %1, off sc1\n\ts_nop 1" :: "v"(p), "v"(v) : "memory"); }
DEVI void ld_async(u32x4& d, const void* p) { asm volatile("global_load_dwordx4 %0, %1, off" : "=v"(d) : "v"(p)); }
DEVI void ld_async(float& d, const void* p) { asm volatile("global_load_dword %0, %1, off" : "=v"(d) : "v"(p)); }
DEVI void ld_async_dev(f32x4& d, const void* p) { asm volatile("global_load_dwordx4 %0, %1, off sc1" : "=v"(d) : "v"(p)); }
DEVI void ld_async_dev(unsigned& d, const void* p) { asm volatile("global_load_dword %0, %1, off sc1" : "=v"(d) : "v"(p)); }
DEVI void st_wt(void* p, float v) { asm volatile("global_store_dword %0, %1, off sc1" :: "v"(p), "v"(v) : "memory"); }
DEVI bf16x8 lds_b128(const LAS char* p) { return *(const LAS bf16x8*)p; }
DEVI s16x4 lds_tr(const LAS char* p) { return __builtin_amdgcn_ds_read_tr16_b64_v4i16((LAS s16x4*)p); }
DEVI bf16x8 cat8(s16x4 a, s16x4 b) { return __builtin_bit_cast(bf16x8, __builtin_shufflevector(a, b, 0, 1, 2, 3, 4, 5, 6, 7)); }
DEVI bf16x8 cat8u(u32x2 a, u32x2 b) { return __builtin_bit_cast(bf16x8, (u32x4){a[0], a[1], b[0], b[1]}); }
DEVI f32x4 mfma(bf16x8 a, bf16x8 b, f32x4 c) { return __builtin_amdgcn_mfma_f32_16x16x32_bf16(a, b, c, 0, 0, 0); }
DEVI void mfma_ip(f32x4& c, bf16x8 a, bf16x8 b) { asm volatile("v_mfma_f32_16x16x32_bf16 %0, %1, %2, %0" : "+v"(c) : "v"(a), "v"(b)); }
template <class T> DEVI void pin(T& v) { asm volatile("" : "+v"(v)); }
DEVI void zero_pin(f32x4& v) { v = (f32x4){0, 0, 0, 0}; pin(v); }
DEVI void mfma_fence() { asm volatile("s_nop 7\n\ts_nop 7" ::: "memory"); }
DEVI unsigned pk_bf16(float lo, float hi) { bf16x2 v = {(__bf16)lo, (__bf16)hi}; return __builtin_bit_cast(unsigned, v); }
DEVI u32x2 pk4(f32x4 v) { return (u32x2){pk_bf16(v[0], v[1]), pk_bf16(v[2], v[3])}; }
DEVI float bf_lo(unsigned u) { return __uint_as_float(u << 16); }
DEVI float bf_hi(unsigned u) { return __uint_as_float(u & 0xffff0000u); }
DEVI float rbf(float x) { return (float)(__bf16)x; }
DEVI float red16_add(float x) { auto s = __builtin_amdgcn_permlane16_swap(__float_as_uint(x), __float_as_uint(x), false, false); return __uint_as_float(s[0]) + __uint_as_float(s[1]); }
DEVI float red32_add(float x) { auto s = __builtin_amdgcn_permlane32_swap(__float_as_uint(x), __float_as_uint(x), false, false); return __uint_as_float(s[0]) + __uint_as_float(s[1]); }
DEVI float red16_max(float x) { auto s = __builtin_amdgcn_permlane16_swap(__float_as_uint(x), __float_as_uint(x), false, false); return fmaxf(__uint_as_float(s[0]), __uint_as_float(s[1])); }
DEVI float red32_max(float x) { auto s = __builtin_amdgcn_permlane32_swap(__float_as_uint(x), __float_as_uint(x), false, false); return fmaxf(__uint_as_float(s[0]), __uint_as_float(s[1])); }
DEVI float ln_rstd(float var) { return 1.0f / rbf(sqrtf(rbf(rbf(var) + 1e-5f))); }

DEVI float wave_sum(float x) {
  x += __builtin_bit_cast(float, __builtin_amdgcn_update_dpp(0, __builtin_bit_cast(int, x), 0xB1, 0xf, 0xf, true));
  x += __builtin_bit_cast(float, __builtin_amdgcn_update_dpp(0, __builtin_bit_cast(int, x), 0x4E, 0xf, 0xf, true));
  x += __builtin_bit_cast(float, __builtin_amdgcn_update_dpp(0, __builtin_bit_cast(int, x), 0x141, 0xf, 0xf, true));
  x += __builtin_bit_cast(float, __builtin_amdgcn_update_dpp(0, __builtin_bit_cast(int, x), 0x140, 0xf, 0xf, true));
  return red32_add(red16_add(x));
}

constexpr int CNT_PF = 64 * 64;
__global__ __launch_bounds__(512) void k0_ln(const u16* __restrict__ x, u16* __restrict__ xn, float* __restrict__ xmean,
                                             const u16* __restrict__ wo, u16* __restrict__ wbar, const u16* __restrict__ wqkv,
                                             unsigned* __restrict__ cnt) {
  const int lane = threadIdx.x & 63, w = threadIdx.x >> 6, row = blockIdx.x * 8 + w;
  TLS(0, 0);
  const u32x4* src = (const u32x4*)(x + (size_t)row * D_);
  const u32x4 a = src[lane], b = src[lane + 64];
  u32x4 wa, wb;
  const bool dow = __builtin_amdgcn_readfirstlane(w) < 4;
  if (dow) { const u32x4* ws = (const u32x4*)(wo + (size_t)(blockIdx.x * 4 + w) * D_); wa = ws[lane]; wb = ws[lane + 64]; }
  unsigned pfv = 0;
  if (lane < 24) pfv = *(const unsigned*)(wqkv + (size_t)(row * 24 + lane) * 64);
  float v[16];
#pragma unroll
  for (int i = 0; i < 4; ++i) { v[2 * i] = bf_lo(a[i]); v[2 * i + 1] = bf_hi(a[i]); v[8 + 2 * i] = bf_lo(b[i]); v[8 + 2 * i + 1] = bf_hi(b[i]); }
  float s = 0.f;
#pragma unroll
  for (int i = 0; i < 16; ++i) s += v[i];
  const float mean = wave_sum(s) * (1.f / D_);
  float q = 0.f;
#pragma unroll
  for (int i = 0; i < 16; ++i) { const float d = v[i] - mean; q += d * d; }
  const float mb = rbf(mean), rs = ln_rstd(wave_sum(q) * (1.f / D_));
  u32x4 oa, ob;
#pragma unroll
  for (int i = 0; i < 4; ++i) {
    const unsigned da = pk_bf16(v[2 * i] - mb, v[2 * i + 1] - mb), db = pk_bf16(v[8 + 2 * i] - mb, v[8 + 2 * i + 1] - mb);
    oa[i] = pk_bf16(bf_lo(da) * rs, bf_hi(da) * rs);
    ob[i] = pk_bf16(bf_lo(db) * rs, bf_hi(db) * rs);
  }
  char* dst = (char*)xn + (size_t)(lane >> 3) * KT_ + row * 128 + (lane & 7) * 16;
  *(u32x4*)dst = oa; *(u32x4*)(dst + 8 * KT_) = ob;
  if (threadIdx.x < 80) cnt[threadIdx.x < 64 ? CNT_PF + blockIdx.x * 64 + threadIdx.x : blockIdx.x * 16 + (threadIdx.x - 64)] = 0u;
  if (lane == 0) xmean[row] = mean;
  if (dow) {
    float t = 0.f;
#pragma unroll
    for (int i = 0; i < 4; ++i) t += (bf_lo(wa[i]) + bf_hi(wa[i])) + (bf_lo(wb[i]) + bf_hi(wb[i]));
    t = wave_sum(t) * (1.f / D_);
    const float hi = rbf(t);
    if (lane < 2) wbar[lane * D_ + blockIdx.x * 4 + w] = (u16)(__float_as_uint(lane ? rbf(t - hi) : hi) >> 16);
  }
  asm volatile("" :: "v"(pfv));
  TLS(0, 3);
}

typedef __attribute__((address_space(1))) unsigned gu32;
DEVI void signal_and_wait(unsigned* add_to, unsigned* wait_on, unsigned target, unsigned* tmo, int lane) {
  if (lane == 0) __hip_atomic_fetch_add((gu32*)add_to, 1u, __ATOMIC_RELAXED, __HIP_MEMORY_SCOPE_AGENT);
  for (unsigned spins = 0;; ++spins) {
    if ((unsigned)__builtin_amdgcn_readfirstlane(__hip_atomic_load((gu32*)wait_on, __ATOMIC_RELAXED, __HIP_MEMORY_SCOPE_AGENT)) >= target) break;
    if (spins > (1u << 15)) { if (lane == 0) __hip_atomic_store((gu32*)tmo, 1u, __ATOMIC_RELAXED, __HIP_MEMORY_SCOPE_AGENT); break; }
    __builtin_amdgcn_s_sleep(1);
  }
}
DEVI void acquire_agent() { asm volatile("" ::: "memory"); }

constexpr int K1_STAGE = 18 * 1024 + 24 * 1024;
constexpr int K1_NS = 3;
constexpr int K1_DUMMY = K1_STAGE * K1_NS;
constexpr int K1_EX = K1_STAGE, K1_OST = 2 * K1_STAGE;
constexpr int P2_W = K1_DUMMY + 1024, P2_NSW = 4;
constexpr int P2_WB = 122880;
constexpr int G_NSA = 4;
constexpr int K3_AH = 16384;
constexpr int K3_UOFF = 15360 + 16;
constexpr int K3_BB = K3_UOFF + 15360;
constexpr int K3_B0 = 4 * K3_AH;
constexpr int K3_TAB = K3_B0 + 3 * K3_BB;
constexpr int K3_DUMMY = K3_TAB + 1024;
constexpr int P4_W = 65536, P4_NSW = 5;
#define P4_D 4
#define P4_PUB 3
constexpr int KM_LDS = 163840;

struct K1Frag { bf16x8 x[4], xh; s16x4 w[3][2][2]; };
struct G64Frag { bf16x8 x[2][2], wb[2]; s16x4 w[4][2][2]; };
struct P4Frag { bf16x8 x[4][2]; s16x4 w[4][2][2]; };

template <int K, int WOFF, int NSW> struct G64Loader {
  i32x4 srdA, srdW; unsigned voffA, voffW, sA, sW, lb;
  DEVI void init(const u16* A, const u16* W, int r0, int c0, int lw, int lane, unsigned lds0) {
    srdA = make_srd(A, S_ * K * 2); srdW = make_srd(W, K * D_ * 2);
    const int lr = lane >> 3, lc = lane & 7, fW = ((lane >> 4) & 1) | ((lw & 1) << 1);
    voffA = (unsigned)((8 * lw + lr) * 128 + ((lc ^ ((4 * lw + (lane >> 4)) & 7)) << 4));
    voffW = (unsigned)((8 * lw + lr) * (D_ * 2) + (((((lc >> 1) ^ fW) << 1) | (lc & 1)) << 4));
    sA = (unsigned)r0 * 128; sW = (unsigned)c0 * 2; lb = lds0 + lw * 1024;
  }
  DEVI void issueW(int t, int slot) const {
#pragma unroll
    for (int j = 0; j < 2; ++j) dma16(lb + WOFF + slot * 8192 + j * 4096, voffW, srdW, sW + (unsigned)(t * 64 + 32 * j) * (D_ * 2));
  }
  DEVI void issueA(int t, int slot) const {
#pragma unroll
    for (int j = 0; j < 4; ++j) dma16_dev(lb + slot * 16384 + j * 4096, voffA, srdA, sA + j * 4096 + t * KT_);
  }
  template <class F> DEVI void run(F&& tail) const {
    constexpr int NT = K / 64;
    static_assert(NSW == 4 && G_NSA == 4, "");
    issueA(0, 0); issueA(1, 1);
    wait_vm<4>(); bar();
    issueA(2, 2); issueA(3, 3);
    wait_vm<8>(); bar(); issueW(4, 0); issueA(4, 0);
    wait_vm<10>(); bar(); issueW(5, 1); issueA(5, 1);
    for (int t = 2; t < NT - 4; ++t) { wait_vm<12>(); bar(); issueW(t + 4, t & 3); issueA(t + 4, t & 3); }
    wait_vm<12>(); bar();
    wait_vm<6>(); bar();
    wait_vm<0>(); bar();
    tail();
  }
};

template <int K, bool LN2, int WOFF, int NSW>
DEVI void g64_compute(LAS char* smem, int w, int lane, int r0, int c0, int tn, const u16* __restrict__ R, u16* __restrict__ O,
                      u16* __restrict__ OC, float* __restrict__ ssq, const float* __restrict__ xmean) {
  constexpr int NT = K / 64;
  const int g = lane >> 4, c = lane & 15;
  const int ecol = c0 + ((g & 1) << 4) + ((g >> 1) << 3);
  const int sw = (lane >> 1) & 7;
  const int aoff0 = (32 * w + c) * 128 + ((g ^ sw) << 4);
  const int q4 = c >> 2, p4 = lane & 3, fR = ((q4 >> 1) & 1) | ((g & 1) << 1);
  const int woff0 = WOFF + (8 * g + q4) * 128 + (fR << 5) + 8 * p4;
  const int wboff = P2_WB + (c & 1) * 2048 + g * 16;
  constexpr int NR = LN2 ? 22 : 20;
  auto rd = [&](G64Frag& f, auto t_c, auto r_c) {
    constexpr int t = decltype(t_c)::v, r = decltype(r_c)::v, ks = r / 10, q = r % 10;
    if constexpr (r >= 20) f.wb[r - 20] = lds_b128(smem + wboff + t * 128 + (r - 20) * 64);
    else if constexpr (q < 2) f.x[q][ks] = lds_b128(smem + (t % G_NSA) * 16384 + (aoff0 ^ (ks << 6)) + q * 2048);
    else f.w[(q - 2) >> 1][ks][q & 1] = lds_tr(smem + (t % NSW) * 8192 + (woff0 ^ (((q - 2) >> 1) << 5)) + ks * 4096 + (q & 1) * 512);
  };
  f32x4 acc[2][4], accm[2];
#pragma unroll
  for (int m = 0; m < 2; ++m) {
    if constexpr (LN2) zero_pin(accm[m]);
#pragma unroll
    for (int j = 0; j < 4; ++j) zero_pin(acc[m][j]);
  }
  constexpr int NM = LN2 ? 20 : 16;
  auto mm = [&](const G64Frag& f, auto i_c) {
    constexpr int i = decltype(i_c)::v, ks = i >> 3, j = (i >> 1) & 3, m = i & 1;
    if constexpr (i < 16) mfma_ip(acc[m][j], cat8(f.w[j][ks][0], f.w[j][ks][1]), f.x[m][ks]);
    else mfma_ip(accm[m], f.wb[j & 1], f.x[m][j & 1]);
  };
  G64Frag fr[2];
  u32x4 res[2][2]; float xm[2];
  bar();
  TLS(LN2 ? 2 : 4, 1);
  sfor<0, NR>([&](auto r) { rd(fr[0], IC<0>{}, r); });
  mfma_fence(); SB();
  sfor<0, NT>([&](auto t_c) {
    constexpr int t = decltype(t_c)::v;
    if constexpr (t < NT - 1) { wait_lgkm0(); bar(); SB(); }
    if constexpr (t == 4) {
#pragma unroll
      for (int m = 0; m < 2; ++m) {
#pragma unroll
        for (int jp = 0; jp < 2; ++jp) ld_async(res[m][jp], R + (size_t)(r0 + 32 * w + 16 * m + c) * D_ + ecol + 32 * jp);
        ld_async(xm[m], xmean + r0 + 32 * w + 16 * m + c);
      }
      SB();
    }
    sfor<0, NM>([&](auto i) {
      constexpr int ii = decltype(i)::v;
      if constexpr (t < NT - 1 && 2 * ii < NR) { rd(fr[(t + 1) & 1], IC<t + 1>{}, IC<2 * ii>{}); rd(fr[(t + 1) & 1], IC<t + 1>{}, IC<2 * ii + 1>{}); }
      mm(fr[t & 1], i);
      SB();
    });
  });
  mfma_fence();
  asm volatile("s_waitcnt vmcnt(0)" : "+v"(res[0][0]), "+v"(res[0][1]), "+v"(res[1][0]), "+v"(res[1][1]), "+v"(xm[0]), "+v"(xm[1]) :: "memory");
  SB();
  TLS(LN2 ? 2 : 4, 2);
  LAS char* st = smem + ((NT - 2) % G_NSA) * 16384 + w * 4096;
#pragma unroll
  for (int m = 0; m < 2; ++m) {
    const int row = r0 + 32 * w + 16 * m + c;
    float mean = 0.f, mb = 0.f, q = 0.f;
    if constexpr (LN2) { mean = xm[m] + (accm[m][0] + accm[m][1]); mb = rbf(mean); }
#pragma unroll
    for (int jp = 0; jp < 2; ++jp) {
      const u32x2 a0 = pk4(acc[m][2 * jp]), a1 = pk4(acc[m][2 * jp + 1]);
      auto e0 = __builtin_amdgcn_permlane16_swap(a0[0], a1[0], false, false);
      auto e1 = __builtin_amdgcn_permlane16_swap(a0[1], a1[1], false, false);
      const unsigned e[4] = {e0[0], e1[0], e0[1], e1[1]};
      u32x4 ov, oc;
#pragma unroll
      for (int i = 0; i < 4; ++i) {
        ov[i] = pk_bf16(bf_lo(e[i]) + bf_lo(res[m][jp][i]), bf_hi(e[i]) + bf_hi(res[m][jp][i]));
        if constexpr (LN2) {
          const float h0 = bf_lo(ov[i]), h1 = bf_hi(ov[i]);
          oc[i] = pk_bf16(h0 - mb, h1 - mb);
          q += (h0 - mean) * (h0 - mean) + (h1 - mean) * (h1 - mean);
        }
      }
      if constexpr (LN2) *(LAS u32x4*)(st + (16 * m + c) * 128 + (((4 * jp + 2 * (g & 1) + (g >> 1)) ^ (c & 7)) << 4)) = oc;
      *(u32x4*)(O + (size_t)row * D_ + ecol + 32 * jp) = ov;
    }
    if constexpr (LN2) {
      q = red32_add(red16_add(q));
      if (g == 0) st_wt(ssq + tn * S_ + row, q);
    }
  }
  if constexpr (LN2) {
#pragma unroll
    for (int i = 0; i < 4; ++i) {
      const int rr = 8 * i + (lane >> 3);
      st_wt((char*)OC + (size_t)tn * KT_ + (r0 + 32 * w + rr) * 128 + (((lane ^ rr) & 7) << 4), *(const LAS u32x4*)(st + i * 1024 + lane * 16));
    }
  }
  TLS(LN2 ? 2 : 4, 3);
}

__global__ __launch_bounds__(512, 2) void km_block(const u16* __restrict__ x, const u16* __restrict__ wqkv, const u16* __restrict__ wo,
                                                   const u16* __restrict__ wg, const u16* __restrict__ wu, const u16* __restrict__ wd,
                                                   u16* __restrict__ out, char* __restrict__ ws) {
  __shared__ __attribute__((aligned(16))) char smem_[KM_LDS];
  LAS char* smem = (LAS char*)smem_;
  const unsigned lds0 = (unsigned)(size_t)smem;
  const int tid = threadIdx.x, lane = tid & 63, w = __builtin_amdgcn_readfirstlane(tid >> 6);
  const int bid = blockIdx.x, xcd = bid & 7, idx = bid >> 3;
  const int tm = 4 * (xcd >> 1) + (idx & 3), tn = 8 * (xcd & 1) + (idx >> 2);
  const int r0 = tm * 128;
  const int tm3 = 4 * (xcd >> 2) + (idx & 3), tn3 = 8 * (xcd & 3) + (idx >> 2);
  u16* const hc = (u16*)(ws + WS_HC); u16* const o = (u16*)(ws + WS_O); u16* const h = (u16*)(ws + WS_H);
  float* const ssq = (float*)(ws + WS_SSQ); const float* const xmean = (const float*)(ws + WS_XMEAN);
  const u16* const wbar = (const u16*)(ws + WS_WBAR); u16* const act = (u16*)(ws + WS_ACT); const u16* const xn = (const u16*)(ws + WS_XN);
  unsigned* const cnt = (unsigned*)(ws + WS_CNT);
  const int w4 = w & 3;
  const int lr = lane >> 3, lc = lane & 7;
  const int g = lane >> 4, c = lane & 15;
  TLS(1, 0);
  auto p3_voffB = [&](int j) {
    const int ch = (w4 + 4 * j) * 64 + lane, gr = ch / 120, r = ch - gr * 120, rr = r / 14, pos = r - rr * 14;
    return r < 112 ? (unsigned)((8 * gr + rr) * (F_ * 2) + (pos << 4)) : OOB;
  };
  const unsigned sB3 = (unsigned)tn3 * 224u;

  if (w >= 4) {
    const int lw = w4;
    {
      constexpr int NT = D_ / 64;
      const i32x4 srdA = make_srd(xn, S_ * D_ * 2), srdW = make_srd(wqkv, D_ * 3 * D_ * 2);
      const unsigned voffA = (unsigned)((8 * lw + lr) * 128 + ((lc ^ ((4 * lw + (lane >> 4)) & 7)) << 4));
      const int fW = ((lane >> 4) & 1) | ((lw & 1) << 1);
      const unsigned voffW = (unsigned)((8 * lw + lr) * (3 * D_ * 2) + (((((lc >> 1) ^ fW) << 1) | (lc & 1)) << 4));
      const unsigned sA = (unsigned)r0 * 128, sW = (unsigned)tn * 128;
      const bool halo = lw < 2;
      const unsigned sX = (unsigned)(r0 >= 16 ? r0 - 16 : 0) * 128;
      auto issue = [&](int t, int slot) {
        const unsigned lb = lds0 + slot * K1_STAGE + lw * 1024;
#pragma unroll
        for (int j = 0; j < 2; ++j) dma16(lb + 18432 + j * 4096, voffW, srdW, sW + (unsigned)(t * 64 + 32 * j) * (3 * D_ * 2));
#pragma unroll
        for (int j = 0; j < 4; ++j) dma16(lb + j * 4096, voffA, srdA, sA + j * 4096 + t * KT_);
#pragma unroll
        for (int s = 1; s < 3; ++s)
#pragma unroll
          for (int j = 0; j < 2; ++j) dma16(lb + 18432 + s * 8192 + j * 4096, voffW, srdW, sW + (unsigned)(t * 64 + 32 * j) * (3 * D_ * 2) + s * (D_ * 2));
        if (halo) dma16(lb + 16384, voffA, srdA, sX + t * KT_);
      };
      issue(0, 0); issue(1, 1);
      wait_vm<10>(); bar();
      issue(2, 2);
      int slot = 0;
      for (int t = 0; t < NT - 3; ++t) {
        wait_vm<10>(); bar();
        issue(t + 3, slot);
        slot = slot == 2 ? 0 : slot + 1;
      }
    }
    G64Loader<D_, P2_W, P2_NSW> L2;
    L2.init(o, wo, r0, tn * 64, lw, lane, lds0);
    wait_vm<10>(); bar();
#pragma unroll
    for (int s = 0; s < P2_NSW; ++s) L2.issueW(s, s);
    wait_vm<2 * P2_NSW>(); bar();
    {
      const i32x4 srdP = make_srd((lw & 1) ? wu : wg, D_ * F_ * 2);
      const unsigned o_ = (unsigned)bid * 28672u + (lw >> 1) * 14336u;
      dma16(lds0 + K1_DUMMY, (unsigned)lane * 128u, srdP, o_);
      dma16(lds0 + K1_DUMMY, lane < 48 ? (unsigned)lane * 128u : OOB, srdP, o_ + 8192u);
    }
    bar(); bar();
    dma16(lds0 + P2_WB + lw * 1024, (unsigned)lane * 16u, make_srd(wbar, 4096), (unsigned)lw * 1024u);
    bar();
    bar();
    acquire_agent();
    L2.run([&]() {
      const i32x4 srdG = make_srd(wg, D_ * F_ * 2), srdU = make_srd(wu, D_ * F_ * 2);
#pragma unroll
      for (int j = 0; j < 4; ++j) {
        const unsigned vo = p3_voffB(j);
        if (j < 3 || lw < 3) {
          dma16(lds0 + K3_B0 + lw * 1024 + j * 4096, vo, srdG, sB3);
          dma16(lds0 + K3_B0 + K3_UOFF + lw * 1024 + j * 4096, vo, srdU, sB3);
          dma16(lds0 + K3_B0 + K3_BB + lw * 1024 + j * 4096, vo, srdG, sB3 + 64 * F_ * 2);
        }
      }
    });
  } else {
    constexpr int NT = D_ / 64;
    const int hh = w & 1, rh = w >> 1;
    const int sw = (lane >> 1) & 7;
    const int aoff0 = (64 * rh + c) * 128 + ((g ^ sw) << 4), hoff0 = (128 + c) * 128 + ((g ^ sw) << 4);
    const int q4 = c >> 2, p4 = lane & 3, fR = ((q4 >> 1) & 1) | ((g & 1) << 1);
    const int woff0 = 18432 + (8 * g + q4) * 128 + (((2 * hh + rh) ^ fR) << 5) + 8 * p4;
    auto rd = [&](K1Frag& f, int slot, int ks, auto r_c) {
      constexpr int r = decltype(r_c)::v;
      const LAS char* b = smem + slot * K1_STAGE;
      if constexpr (r >= 2 && r <= 5) f.x[r - 2] = lds_b128(b + (aoff0 ^ (ks << 6)) + (r - 2) * 2048);
      else if constexpr (r == 12) f.xh = lds_b128(b + (hoff0 ^ (ks << 6)));
      else {
        constexpr int rr = r < 2 ? r : (r < 12 ? r - 4 : r - 5);
        constexpr int mat = rr >> 2, sl = (rr >> 1) & 1, hf = rr & 1;
        f.w[mat][sl][hf] = lds_tr(b + (woff0 ^ (sl << 5)) + ks * 4096 + mat * 8192 + hf * 512);
      }
    };
    f32x4 aq[4][2], ak[4][2], av[4][2], ahk, ahv;
#pragma unroll
    for (int m = 0; m < 4; ++m)
#pragma unroll
      for (int s = 0; s < 2; ++s) { zero_pin(aq[m][s]); zero_pin(ak[m][s]); zero_pin(av[m][s]); }
    zero_pin(ahk); zero_pin(ahv);
    constexpr int NM = 26;
    auto mm = [&](const K1Frag& f, auto i_c) {
      constexpr int i = decltype(i_c)::v;
      if constexpr (i < 8) mfma_ip(aq[i & 3][i >> 2], cat8(f.w[0][i >> 2][0], f.w[0][i >> 2][1]), f.x[i & 3]);
      else if constexpr (i < 16) mfma_ip(ak[i & 3][(i - 8) >> 2], cat8(f.w[1][(i - 8) >> 2][0], f.w[1][(i - 8) >> 2][1]), f.x[i & 3]);
      else if constexpr (i == 16) mfma_ip(ahk, cat8(f.w[1][0][0], f.w[1][0][1]), f.xh);
      else if constexpr (i < 25) mfma_ip(av[(i - 17) & 3][(i - 17) >> 2], f.x[(i - 17) & 3], cat8(f.w[2][(i - 17) >> 2][0], f.w[2][(i - 17) >> 2][1]));
      else mfma_ip(ahv, f.xh, cat8(f.w[2][0][0], f.w[2][0][1]));
    };
    K1Frag fa, fb;
    __builtin_amdgcn_s_setprio(1);
    bar();
    TLS(1, 1);
    sfor<0, 17>([&](auto r) { rd(fa, 0, 0, r); });
    mfma_fence(); SB();
    constexpr int NB = 4;
    sfor<0, NT>([&](auto t_c) {
      constexpr int t = decltype(t_c)::v, slot = t % K1_NS;
      constexpr bool last = t == NT - 1;
      sfor<0, NM>([&](auto i) { mm(fa, i); if constexpr (decltype(i)::v < 17) rd(fb, slot, 1, i); SB(); });
      sfor<0, NB>([&](auto i) { mm(fb, i); SB(); });
      if constexpr (!last) { wait_lgkm0(); bar(); SB(); }
      sfor<NB, NM>([&](auto i) {
        mm(fb, i);
        if constexpr (!last && decltype(i)::v - NB < 17) rd(fa, (slot + 1) % K1_NS, 0, IC<decltype(i)::v - NB>{});
        SB();
      });
    });
    mfma_fence(); SB();
    TLS(1, 2);
    LAS char* ex = smem + K1_EX;
    auto exa = [&](int h_, int r_, int kind, int b) { return (LAS u32x2*)(ex + ((((h_ * 2 + r_) * 2 + kind) * 2 + b) * 64 + lane) * 8); };
    u32x2 qp[4][2], kp[4][2], vp[4][2];
#pragma unroll
    for (int s = 0; s < 2; ++s) { kp[3][s] = pk4(ak[3][s]); vp[3][s] = pk4(av[3][s]); }
    *exa(hh, 0, 0, rh) = pk4(ahk); *exa(hh, 0, 1, rh) = pk4(ahv);
    if (rh == 0) {
      *exa(hh, 1, 0, 0) = kp[3][0]; *exa(hh, 1, 0, 1) = kp[3][1];
      *exa(hh, 1, 1, 0) = vp[3][0]; *exa(hh, 1, 1, 1) = vp[3][1];
    }
#pragma unroll
    for (int m = 0; m < 4; ++m)
#pragma unroll
      for (int s = 0; s < 2; ++s) { qp[m][s] = pk4(aq[m][s]); if (m < 3) { kp[m][s] = pk4(ak[m][s]); vp[m][s] = pk4(av[m][s]); } }
    wait_lgkm0(); bar();
    u32x2 kprev[2], vprev[2];
#pragma unroll
    for (int s = 0; s < 2; ++s) { kprev[s] = *exa(hh, rh, 0, s ^ rh); vprev[s] = *exa(hh, rh, 1, s ^ rh); }
    const bool odd = c & 1;
    const int d1 = c - 4 * g - (c & 1);
    const float NINF = -__builtin_inff();
    const float b0a = d1 + 16 <= 14 ? 0.f : NINF, b0b = d1 + 14 <= 14 ? 0.f : NINF, b1a = d1 >= 0 ? 0.f : NINF, b1b = d1 - 2 >= 0 ? 0.f : NINF;
    const bool first = (r0 + 64 * rh) == 0;
    const unsigned pmask = odd ? 0xffff0000u : 0x0000ffffu;
    const bf16x8 ones = __builtin_bit_cast(bf16x8, (u32x4){0x3f803f80u, 0x3f803f80u, 0x3f803f80u, 0x3f803f80u});
    const float SL2E = 0.17677669529663687f * 1.4426950408889634f;
    const int och = 4 * hh + 2 * ((g & 1) ^ rh) + (g >> 1);
    LAS char* ost = smem + K1_OST + (64 * rh + c) * 128 + ((och ^ (c & 7)) << 4);
#pragma unroll
    for (int mi = 0; mi < 4; ++mi) {
      const int m = (mi + 1) & 3;
      const bf16x8 qf = cat8u(qp[m][0], qp[m][1]);
      const f32x4 s0 = mfma(m ? cat8u(kp[m - 1][0], kp[m - 1][1]) : cat8u(kprev[0], kprev[1]), qf, (f32x4){0, 0, 0, 0});
      const f32x4 s1 = mfma(cat8u(kp[m][0], kp[m][1]), qf, (f32x4){0, 0, 0, 0});
      const float l0 = fmaf(odd ? s0[1] : s0[0], SL2E, (m == 0 && first) ? NINF : b0a);
      const float l1 = fmaf(odd ? s0[3] : s0[2], SL2E, (m == 0 && first) ? NINF : b0b);
      const float l2 = fmaf(odd ? s1[1] : s1[0], SL2E, b1a);
      const float l3 = fmaf(odd ? s1[3] : s1[2], SL2E, b1b);
      const float mx = red32_max(red16_max(fmaxf(fmaxf(l0, l1), fmaxf(l2, l3))));
      const float p0 = __builtin_amdgcn_exp2f(l0 - mx), p1 = __builtin_amdgcn_exp2f(l1 - mx), p2 = __builtin_amdgcn_exp2f(l2 - mx), p3 = __builtin_amdgcn_exp2f(l3 - mx);
      const bf16x8 pf = __builtin_bit_cast(bf16x8, (u32x4){pk_bf16(p0, p0) & pmask, pk_bf16(p1, p1) & pmask, pk_bf16(p2, p2) & pmask, pk_bf16(p3, p3) & pmask});
      const f32x4 sum = mfma(ones, pf, (f32x4){0, 0, 0, 0});
      const f32x4 o0 = mfma(cat8u(m ? vp[m - 1][0] : vprev[0], vp[m][0]), pf, (f32x4){0, 0, 0, 0});
      const f32x4 o1 = mfma(cat8u(m ? vp[m - 1][1] : vprev[1], vp[m][1]), pf, (f32x4){0, 0, 0, 0});
      const float inv = __builtin_amdgcn_rcpf(sum[0]);
      const u32x2 op0 = pk4(o0 * inv), op1 = pk4(o1 * inv);
      auto e0 = __builtin_amdgcn_permlane16_swap(op0[0], op1[0], false, false);
      auto e1 = __builtin_amdgcn_permlane16_swap(op0[1], op1[1], false, false);
      *(LAS u32x4*)(ost + m * 2048) = (u32x4){e0[0], e1[0], e0[1], e1[1]};
    }
    wait_lgkm0(); bar();
#pragma unroll
    for (int i = 0; i < 4; ++i) {
      const int rr = 32 * w + 8 * i + lr;
      st_wt((char*)o + (size_t)tn * KT_ + (r0 + rr) * 128 + (((lane ^ rr) & 7) << 4), *(const LAS u32x4*)(smem + K1_OST + (4 * w + i) * 1024 + lane * 16));
    }
    wait_lgkm0(); wait_vm<0>();
    TLS(1, 3);
    bar();
    if (w == 0) signal_and_wait(cnt + tm * 64, cnt + tm * 64, 16, cnt + 32 * 64, lane);
    bar();
    TLS(2, 0);
    g64_compute<D_, true, P2_W, P2_NSW>(smem, w, lane, r0, tn * 64, tn, x, h, hc, ssq, xmean);
    wait_lgkm0(); wait_vm<0>();
    __builtin_amdgcn_s_setprio(0);
  }
  bar();
  if (w == 0) signal_and_wait(cnt + (16 + (tm >> 1)) * 64, cnt + (16 + tm3) * 64, 32, cnt + 32 * 64, lane);
  bar();
  acquire_agent();
  {
    TLS(3, 0);
    const int grp = w >> 2, wm = (w >> 1) & 1, wn = w & 1;
    const int r3 = tm3 * 256;
    const i32x4 srdA = make_srd(hc, S_ * D_ * 2), srdB = make_srd(grp ? wu : wg, D_ * F_ * 2);
    const unsigned voffA = (unsigned)((8 * w4 + lr) * 128 + ((lc ^ ((4 * w4 + (lane >> 4)) & 7)) << 4));
    const unsigned sA = (unsigned)r3 * 128;
    auto dmaA = [&](int t, int half, int buf, int j) {
      dma16_dev(lds0 + (half * 2 + buf) * K3_AH + w4 * 1024 + j * 4096, voffA, srdA, sA + half * 16384 + j * 4096 + t * KT_);
    };
#pragma unroll
    for (int j = 0; j < 4; ++j) dmaA(0, grp, 0, j);
    const bool pfw = w4 == 3;
    const i32x4 srdB3 = make_srd(pfw ? wd : (grp ? wu : wg), D_ * F_ * 2);
    unsigned voffB[4];
#pragma unroll
    for (int j = 0; j < 4; ++j) voffB[j] = p3_voffB(j);
    if (pfw) voffB[3] = lane < 8 ? (unsigned)lane * 128u : OOB;
    auto dmaB = [&](int t, int buf, int j) {
      const unsigned lb = lds0 + K3_B0 + buf * K3_BB + grp * K3_UOFF + w4 * 1024 + j * 4096, so = sB3 + (unsigned)(t * 64) * (F_ * 2);
      if (j < 3) dma16(lb, voffB[j], srdB, so);
      else dma16(pfw ? lds0 + K3_DUMMY : lb, voffB[3], srdB3, pfw ? (unsigned)bid * 28672u + (unsigned)(2 * (t - 2) + grp) * 1024u : so);
    };
    const int sw = (lane >> 1) & 7;
    const int aoff0 = grp * 2 * K3_AH + (64 * wm + c) * 128 + ((g ^ sw) << 4);
    const int q4 = c >> 2, p4 = lane & 3;
    const int boffP = K3_B0 + g * 1920 + q4 * 224 + wn * 112 + 8 * p4;
    const int boffM = K3_B0 + g * 1920 + q4 * 224 + wn * 112 + 96 + (p4 >> 1) * K3_UOFF + 8 * (p4 & 1);
    bf16x8 fx[4][2]; s16x4 fw[7][2][2];
    auto rd = [&](int abuf, int bbuf, auto r_c) {
      constexpr int r = decltype(r_c)::v, ks = r / 18, q = r % 18;
      if constexpr (q < 14) {
        constexpr int j = q >> 1, hf = q & 1;
        fw[j][ks][hf] = lds_tr(smem + (j == 6 ? boffM : boffP) + bbuf * K3_BB + (j < 3 ? j * 32 : (j < 6 ? K3_UOFF + (j - 3) * 32 : 0)) + ks * 7680 + hf * 896);
      } else fx[q - 14][ks] = lds_b128(smem + (aoff0 ^ (ks << 6)) + abuf * K3_AH + (q - 14) * 2048);
    };
    f32x4 acc[4][7];
#pragma unroll
    for (int m = 0; m < 4; ++m)
#pragma unroll
      for (int j = 0; j < 7; ++j) zero_pin(acc[m][j]);
    auto mma = [&]() {
      __builtin_amdgcn_s_setprio(1);
#pragma unroll
      for (int ks = 0; ks < 2; ++ks)
#pragma unroll
        for (int j = 0; j < 7; ++j)
#pragma unroll
          for (int m = 0; m < 4; ++m) mfma_ip(acc[m][j], cat8(fw[j][ks][0], fw[j][ks][1]), fx[m][ks]);
      __builtin_amdgcn_s_setprio(0);
    };
    constexpr int NT = D_ / 64;
    if (grp == 0) {
      wait_vm<0>(); bar();
    } else {
      wait_vm<4>(); bar();
      float st[16];
      const float* sp = ssq + r3 + (tid & 255);
#pragma unroll
      for (int i = 0; i < 16; ++i) asm volatile("global_load_dword %0, %1, off sc1" : "=v"(st[i]) : "v"(sp + i * S_));
#pragma unroll
      for (int j = 0; j < 4; ++j) { dmaA(1, 0, 1, j); dmaB(1, 1, j); }
      asm volatile("s_waitcnt vmcnt(8)" : "+v"(st[0]), "+v"(st[1]), "+v"(st[2]), "+v"(st[3]), "+v"(st[4]), "+v"(st[5]), "+v"(st[6]), "+v"(st[7]),
                   "+v"(st[8]), "+v"(st[9]), "+v"(st[10]), "+v"(st[11]), "+v"(st[12]), "+v"(st[13]), "+v"(st[14]), "+v"(st[15]) :: "memory");
      float q = 0.f;
#pragma unroll
      for (int i = 0; i < 16; ++i) q += st[i];
      *(LAS float*)(smem + K3_TAB + (tid & 255) * 4) = ln_rstd(q * (1.f / D_));
      wait_lgkm0(); bar();
    }
    mfma_fence(); SB();
    const int ahalf = grp ^ 1;
    sfor<0, NT>([&](auto t_c) {
      constexpr int t = decltype(t_c)::v, ab = t & 1, bb = t % 3;
      const int abuf = ab ^ ahalf;
      sfor<0, 36>([&](auto r) {
        constexpr int rr = decltype(r)::v;
        rd(ab, bb, r);
        if constexpr (t < NT - 2 && (rr & 3) == 3 && rr < 32) {
          constexpr int d = rr >> 2;
          if constexpr (d & 1) dmaB(t + 2, (bb + 2) % 3, d >> 1); else dmaA(t + 1 + grp, ahalf, abuf, d >> 1);
        }
        if constexpr (t == NT - 2 && (rr & 7) == 3 && rr < 32) { if (grp == 0) dmaA(t + 1, 1, ab ^ 1, rr >> 3); }
        if constexpr (t < NT - 2) SB();
      });
      wait_lgkm0();
      if constexpr (t < NT - 2) wait_vm<8>();
      else if constexpr (t == NT - 2) { if (grp == 0) wait_vm<4>(); else wait_vm<0>(); }
      else wait_vm<0>();
      bar(); SB();
      mma();
      SB();
      if constexpr (t < NT - 1) bar(); else { if (grp == 0) bar(); }
      SB();
    });
    mfma_fence(); SB();
    TLS(3, 2);
    const float C0 = -2.0f * 1.4426950408889634f * 0.7978845608028654f, C1 = C0 * 0.044715f;
    auto geglu2 = [&](f32x2 gt, f32x2 up, float c0a, float c1a, float ia2) {
      const f32x2 z = gt * (gt * gt * c1a + c0a);
      f32x2 e; e.x = __builtin_amdgcn_exp2f(z.x); e.y = __builtin_amdgcn_exp2f(z.y);
      const f32x2 d = e * ia2 + ia2;
      f32x2 r; r.x = __builtin_amdgcn_rcpf(d.x); r.y = __builtin_amdgcn_rcpf(d.y);
      const f32x2 o2 = gt * up * r;
      return pk_bf16(o2.x, o2.y);
    };
    const int rowb = 128 * grp + 64 * wm;
    LAS char* ast = smem + w * 8192 + c * 128 + 8 * (g & 1);
    const int csw = c & 7;
#pragma unroll
    for (int mp = 0; mp < 2; ++mp) {
      const int mrow = 2 * mp + (g >> 1);
      const float a = *(const LAS float*)(smem + K3_TAB + (rowb + 16 * mrow + c) * 4);
      const float c0a = C0 * a, c1a = C1 * a * a * a, ia2 = __builtin_amdgcn_rcpf(a * a);
      float gt[4], up[4];
#pragma unroll
      for (int r = 0; r < 4; ++r) {
        auto s = __builtin_amdgcn_permlane32_swap(__float_as_uint(acc[2 * mp][6][r]), __float_as_uint(acc[2 * mp + 1][6][r]), false, false);
        gt[r] = __uint_as_float(s[0]); up[r] = __uint_as_float(s[1]);
      }
      *(LAS u32x2*)(ast + mrow * 2048 + ((6 ^ csw) << 4)) =
          (u32x2){geglu2((f32x2){gt[0], gt[1]}, (f32x2){up[0], up[1]}, c0a, c1a, ia2), geglu2((f32x2){gt[2], gt[3]}, (f32x2){up[2], up[3]}, c0a, c1a, ia2)};
      *(LAS u32x2*)(ast + mrow * 2048 + ((7 ^ csw) << 4)) = (u32x2){0u, 0u};
    }
    char* actw = (char*)act + (size_t)(2 * tn3 + wn) * KT_ + (r3 + rowb + lr) * 128 + ((lc ^ lr) << 4);
#pragma unroll
    for (int m = 0; m < 4; ++m) {
      const float a = *(const LAS float*)(smem + K3_TAB + (rowb + 16 * m + c) * 4);
      const float c0a = C0 * a, c1a = C1 * a * a * a, ia2 = __builtin_amdgcn_rcpf(a * a);
#pragma unroll
      for (int b = 0; b < 3; ++b) {
        const f32x4 gt = acc[m][b], up = acc[m][3 + b];
        *(LAS u32x2*)(ast + m * 2048 + (((2 * b + (g >> 1)) ^ csw) << 4)) =
            (u32x2){geglu2((f32x2){gt[0], gt[1]}, (f32x2){up[0], up[1]}, c0a, c1a, ia2), geglu2((f32x2){gt[2], gt[3]}, (f32x2){up[2], up[3]}, c0a, c1a, ia2)};
      }
#pragma unroll
      for (int i = 0; i < 2; ++i) st_wt(actw + (2 * m + i) * 1024, *(const LAS u32x4*)(smem + w * 8192 + (2 * m + i) * 1024 + lane * 16));
    }
    TLS(3, 3);
  }
  {
    constexpr int NX = 32 - P4_D, NS4 = 32 + P4_D;
    const int kh = tn & 1;
    const i32x4 srdW = make_srd(wd, F_ * D_ * 2);
    const int fW = ((lane >> 4) & 1) | ((w4 & 1) << 1);
    const unsigned voffW0 = (unsigned)((8 * w4 + lr) * (D_ * 2) + (((((lc >> 1) ^ fW) << 1) | (lc & 1)) << 4));
    const bool w7 = w4 != 3;
    const unsigned stx = kh ? (unsigned)NS4 : 0u;
    auto issueW = [&](auto s_c) {
      constexpr int s = decltype(s_c)::v;
      const unsigned T = s < NX ? s + stx : s, lb = lds0 + P4_W + (s % P4_NSW) * 16384 + w4 * 1024, so = T * (56 * D_ * 2);
      dma16(lb, voffW0, srdW, so + tn * 128);
      if constexpr (s < NX) dma16(lb + 8192, voffW0, srdW, so + (tn ^ 1) * 128);
      if (w7) {
        dma16(lb + 4096, voffW0, srdW, so + 32 * D_ * 2 + tn * 128);
        if constexpr (s < NX) dma16(lb + 12288, voffW0, srdW, so + 32 * D_ * 2 + (tn ^ 1) * 128);
      }
    };
    wait_lgkm0(); wait_vm<0>();
    if (w < 4) {
      sfor<0, P4_NSW>([&](auto s) { issueW(s); });
      for (int i = w; i < 2 * P4_NSW; i += 4) *(LAS u32x4*)(smem + P4_W + i * 8192 + 7168 + lane * 16) = (u32x4){0u, 0u, 0u, 0u};
      wait_lgkm0();
    }
    bar();
    if (w == 4) signal_and_wait(cnt + (24 + tm3) * 64, cnt + (24 + (tm >> 1)) * 64, 32, cnt + 32 * 64, lane);
    bar();
    TLS(4, 0);
    if (w >= 4) {
      acquire_agent();
      const i32x4 srdA = make_srd(act, 64 * KT_);
      const unsigned voffA = (unsigned)((8 * w4 + lr) * 128 + ((lc ^ ((4 * w4 + (lane >> 4)) & 7)) << 4)), sA = (unsigned)r0 * 128;
      auto issueA = [&](auto s_c) {
        constexpr int s = decltype(s_c)::v;
        const unsigned T = s < NX ? s + stx : s;
#pragma unroll
        for (int j = 0; j < 4; ++j) dma16_dev(lds0 + (s % G_NSA) * 16384 + w4 * 1024 + j * 4096, voffA, srdA, sA + j * 4096 + T * KT_);
      };
      issueA(IC<0>{}); issueA(IC<1>{});
      wait_vm<4>(); bar();
      issueA(IC<2>{}); issueA(IC<3>{});
      sfor<0, NS4 - 1>([&](auto s_c) {
        constexpr int s = decltype(s_c)::v;
        constexpr auto nw = [](int st) { return (st < 0 || st + P4_NSW >= NS4) ? 0 : (st + P4_NSW < NX ? 2 : 1); };
        constexpr int n = nw(s - 2) + nw(s - 1) + (s + 2 < NS4 ? 4 : 0) + (s + 3 < NS4 ? 4 : 0);
        wait_vm<n>(); bar();
        if constexpr (s + P4_NSW < NS4) issueW(IC<s + P4_NSW>{});
        if constexpr (s + 4 < NS4) issueA(IC<s + 4>{});
      });
    } else {
      __builtin_amdgcn_s_setprio(1);
      const int wm = w >> 1, wo_ = w & 1;
      const int sw = (lane >> 1) & 7;
      const int aoff0 = (64 * wm + c) * 128 + ((g ^ sw) << 4);
      const int q4 = c >> 2, p4 = lane & 3, fR = ((q4 >> 1) & 1) | ((g & 1) << 1);
      const int woff0 = P4_W + wo_ * 8192 + (8 * g + q4) * 128 + (fR << 5) + 8 * p4;
      auto rd = [&](P4Frag& f, auto s_c, auto r_c) {
        constexpr int s = decltype(s_c)::v, r = decltype(r_c)::v, ks = r / 12, q = r % 12;
        if constexpr (q < 4) f.x[q][ks] = lds_b128(smem + (s % G_NSA) * 16384 + (aoff0 ^ (ks << 6)) + q * 2048);
        else f.w[(q - 4) >> 1][ks][q & 1] = lds_tr(smem + (s % P4_NSW) * 16384 + (woff0 ^ (((q - 4) >> 1) << 5)) + ks * 4096 + (q & 1) * 512);
      };
      f32x4 acc[4][4];
#pragma unroll
      for (int m = 0; m < 4; ++m)
#pragma unroll
        for (int j = 0; j < 4; ++j) zero_pin(acc[m][j]);
      P4Frag fr[2];
      auto step = [&](auto s_c, auto pre_c) {
        constexpr int s = decltype(s_c)::v;
        sfor<0, 32>([&](auto i_c) {
          constexpr int i = decltype(i_c)::v, ks = i >> 4, j = (i >> 2) & 3, m = i & 3;
          if constexpr (decltype(pre_c)::v && i < 24) rd(fr[(s + 1) & 1], IC<s + 1>{}, i_c);
          mfma_ip(acc[m][j], cat8(fr[s & 1].w[j][ks][0], fr[s & 1].w[j][ks][1]), fr[s & 1].x[m][ks]);
          SB();
        });
      };
      wait_vm<0>();
      bar();
      TLS(4, 1);
      sfor<0, 24>([&](auto r) { rd(fr[0], IC<0>{}, r); });
      mfma_fence(); SB();
      sfor<0, NX>([&](auto s) { wait_lgkm0(); bar(); SB(); step(s, IC<1>{}); });
      f32x4* const part = (f32x4*)(ws + WS_PART);
      mfma_fence(); SB();
      if (wo_) {
#pragma unroll
        for (int i = 0; i < 16; ++i) st_wt(part + ((bid * 2 + wm) * 16 + i) * 64 + lane, acc[i >> 2][i & 3]);
        sfor<NX, NS4 - 1>([&](auto s_c) {
          if constexpr (decltype(s_c)::v == NX + P4_PUB) {
            wait_vm<0>();
            if (lane == 0) __hip_atomic_store((gu32*)(cnt + CNT_PF + (bid * 2 + wm) * 32), 1u, __ATOMIC_RELAXED, __HIP_MEMORY_SCOPE_AGENT);
          }
          bar();
        });
      } else {
        sfor<NX, NS4 - 1>([&](auto s) { wait_lgkm0(); bar(); SB(); step(s, IC<1>{}); });
        const int pb = bid ^ 32;
        unsigned* const pf = cnt + CNT_PF + (pb * 2 + wm) * 32;
        unsigned fl;
        ld_async_dev(fl, pf);
        const int ecol = tn * 64 + ((g & 1) << 4) + ((g >> 1) << 3);
        u32x4 res[4][2];
#pragma unroll
        for (int m = 0; m < 4; ++m)
#pragma unroll
          for (int jp = 0; jp < 2; ++jp) ld_async(res[m][jp], h + (size_t)(r0 + 64 * wm + 16 * m + c) * D_ + ecol + 32 * jp);
        SB();
        step(IC<NS4 - 1>{}, IC<0>{});
        mfma_fence();
        asm volatile("s_waitcnt vmcnt(8)" : "+v"(fl) :: "memory");
        TLS(4, 2);
        if (__builtin_amdgcn_readfirstlane(fl) == 0u) {
          for (unsigned spins = 0;; ++spins) {
            if (__builtin_amdgcn_readfirstlane(__hip_atomic_load((gu32*)pf, __ATOMIC_RELAXED, __HIP_MEMORY_SCOPE_AGENT)) != 0u) break;
            if (spins > (1u << 15)) { if (lane == 0) __hip_atomic_store((gu32*)(cnt + 32 * 64), 1u, __ATOMIC_RELAXED, __HIP_MEMORY_SCOPE_AGENT); break; }
            __builtin_amdgcn_s_sleep(1);
          }
        }
        f32x4 pp[16];
#pragma unroll
        for (int i = 0; i < 16; ++i) ld_async_dev(pp[i], part + ((pb * 2 + wm) * 16 + i) * 64 + lane);
        asm volatile("s_waitcnt vmcnt(0)" : "+v"(pp[0]), "+v"(pp[1]), "+v"(pp[2]), "+v"(pp[3]), "+v"(pp[4]), "+v"(pp[5]), "+v"(pp[6]), "+v"(pp[7]),
                     "+v"(pp[8]), "+v"(pp[9]), "+v"(pp[10]), "+v"(pp[11]), "+v"(pp[12]), "+v"(pp[13]), "+v"(pp[14]), "+v"(pp[15]),
                     "+v"(res[0][0]), "+v"(res[0][1]), "+v"(res[1][0]), "+v"(res[1][1]), "+v"(res[2][0]), "+v"(res[2][1]), "+v"(res[3][0]), "+v"(res[3][1]) :: "memory");
#pragma unroll
        for (int m = 0; m < 4; ++m)
#pragma unroll
          for (int jp = 0; jp < 2; ++jp) {
            const u32x2 a0 = pk4(acc[m][2 * jp] + pp[4 * m + 2 * jp]), a1 = pk4(acc[m][2 * jp + 1] + pp[4 * m + 2 * jp + 1]);
            auto e0 = __builtin_amdgcn_permlane16_swap(a0[0], a1[0], false, false);
            auto e1 = __builtin_amdgcn_permlane16_swap(a0[1], a1[1], false, false);
            const unsigned e[4] = {e0[0], e1[0], e0[1], e1[1]};
            u32x4 ov;
#pragma unroll
            for (int i = 0; i < 4; ++i) ov[i] = pk_bf16(bf_lo(e[i]) + bf_lo(res[m][jp][i]), bf_hi(e[i]) + bf_hi(res[m][jp][i]));
            *(u32x4*)(out + (size_t)(r0 + 64 * wm + 16 * m + c) * D_ + ecol + 32 * jp) = ov;
          }
        TLS(4, 3);
      }
    }
  }
}

extern "C" void kernel_launch(void* const* d_in, const int* in_sizes, int n_in, void* d_out, int out_size, void* d_ws, size_t ws_size,
                              hipStream_t stream) {
  const u16* x = (const u16*)d_in[0];
  const u16* wqkv = (const u16*)d_in[1];
  const u16* wo = (const u16*)d_in[2];
  const u16* wg = (const u16*)d_in[4];
  const u16* wu = (const u16*)d_in[5];
  const u16* wd = (const u16*)d_in[6];
  char* ws = (char*)d_ws;
  k0_ln<<<256, 512, 0, stream>>>(x, (u16*)(ws + WS_XN), (float*)(ws + WS_XMEAN), wo, (u16*)(ws + WS_WBAR), wqkv, (unsigned*)(ws + WS_CNT));
  km_block<<<256, 512, 0, stream>>>(x, wqkv, wo, wg, wu, wd, (u16*)d_out, ws);
}
```

```cpp
#include <hip/hip_runtime.h>
#include <hip/hip_bf16.h>

typedef unsigned short u16;
typedef __attribute__((ext_vector_type(8))) __bf16 bf16x8;
typedef __attribute__((ext_vector_type(2))) __bf16 bf16x2;
typedef __attribute__((ext_vector_type(4))) short s16x4;
typedef __attribute__((ext_vector_type(4))) float f32x4;
typedef __attribute__((ext_vector_type(2))) float f32x2;
typedef __attribute__((ext_vector_type(4))) int i32x4;
typedef __attribute__((ext_vector_type(4))) unsigned u32x4;
typedef __attribute__((ext_vector_type(2))) unsigned u32x2;
#define LAS __attribute__((address_space(3)))
#define DEVI __device__ __forceinline__

constexpr int S_ = 2048, D_ = 1024, F_ = 3584;
constexpr size_t WS_HC = 0, WS_O = 4u << 20, WS_H = 8u << 20, WS_SSQ = 12u << 20, WS_XMEAN = 13u << 20, WS_WBAR = 14u << 20, WS_CNT = 15u << 20,
                 WS_ACT = 16u << 20, WS_XN = 32u << 20, WS_PART = 40u << 20;
constexpr unsigned KT_ = S_ * 128;
constexpr unsigned OOB = 0x80000000u;

DEVI void wait_lgkm0() { asm volatile("s_waitcnt lgkmcnt(0)" ::: "memory"); }
template <int N> struct IC { static constexpr int v = N; };
template <int I, int N, class F> DEVI void sfor(F&& f) { if constexpr (I < N) { f(IC<I>{}); sfor<I + 1, N>(f); } }
#define TLR(n) do {} while (0)
#define TLW(k) do {} while (0)
#define TLS(k, i) do {} while (0)

DEVI i32x4 make_srd(const void* p, unsigned bytes) {
  unsigned long long a = (unsigned long long)p;
  i32x4 r;
  r.x = (int)(unsigned)a; r.y = (int)((unsigned)(a >> 32) & 0xffffu); r.z = (int)bytes; r.w = 0x00020000;
  return r;
}
DEVI void dma16(unsigned lds_addr, unsigned voff, i32x4 srd, unsigned soff) {
  asm volatile("s_mov_b32 m0, %0\n\ts_nop 0\n\tbuffer_load_dwordx4 %1, %2, %3 offen lds"
               :: "s"(lds_addr), "v"(voff), "s"(srd), "s"(soff));
}
DEVI void touch_lines(unsigned& sink, unsigned voff, i32x4 srd, unsigned soff) {
  asm volatile("buffer_load_dword %0, %1, %2, %3 offen" : "+v"(sink) : "v"(voff), "s"(srd), "s"(soff));
}
DEVI void dma16_dev(unsigned lds_addr, unsigned voff, i32x4 srd, unsigned soff) {
  asm volatile("s_mov_b32 m0, %0\n\ts_nop 0\n\tbuffer_load_dwordx4 %1, %2, %3 offen sc1 lds"
               :: "s"(lds_addr), "v"(voff), "s"(srd), "s"(soff));
}
template <int N> DEVI void wait_vm() { asm volatile("s_waitcnt vmcnt(%0)" :: "n"(N) : "memory"); }
DEVI void bar() { asm volatile("" ::: "memory"); __builtin_amdgcn_s_barrier(); asm volatile("" ::: "memory"); }
#define SB() __builtin_amdgcn_sched_barrier(0)

DEVI void st_wt(void* p, u32x4 v) { asm volatile("global_store_dwordx4 %0, %1, off sc1\n\ts_nop 1" :: "v"(p), "v"(v) : "memory"); }
DEVI void st_wt(void* p, f32x4 v) { asm volatile("global_store_dwordx4 %0, %1, off sc1\n\ts_nop 1" :: "v"(p), "v"(v) : "memory"); }
DEVI void ld_async(u32x4& d, const void* p) { asm volatile("global_load_dwordx4 %0, %1, off" : "=v"(d) : "v"(p)); }
DEVI void ld_async(float& d, const void* p) { asm volatile("global_load_dword %0, %1, off" : "=v"(d) : "v"(p)); }
DEVI void ld_async_dev(f32x4& d, const void* p) { asm volatile("global_load_dwordx4 %0, %1, off sc1" : "=v"(d) : "v"(p)); }
DEVI void ld_async_dev(unsigned& d, const void* p) { asm volatile("global_load_dword %0, %1, off sc1" : "=v"(d) : "v"(p)); }
DEVI void st_wt(void* p, float v) { asm volatile("global_store_dword %0, %1, off sc1" :: "v"(p), "v"(v) : "memory"); }
DEVI bf16x8 lds_b128(const LAS char* p) { return *(const LAS bf16x8*)p; }
DEVI s16x4 lds_tr(const LAS char* p) { return __builtin_amdgcn_ds_read_tr16_b64_v4i16((LAS s16x4*)p); }
DEVI bf16x8 cat8(s16x4 a, s16x4 b) { return __builtin_bit_cast(bf16x8, __builtin_shufflevector(a, b, 0, 1, 2, 3, 4, 5, 6, 7)); }
DEVI bf16x8 cat8u(u32x2 a, u32x2 b) { return __builtin_bit_cast(bf16x8, (u32x4){a[0], a[1], b[0], b[1]}); }
DEVI f32x4 mfma(bf16x8 a, bf16x8 b, f32x4 c) { return __builtin_amdgcn_mfma_f32_16x16x32_bf16(a, b, c, 0, 0, 0); }
DEVI void mfma_ip(f32x4& c, bf16x8 a, bf16x8 b) { asm volatile("v_mfma_f32_16x16x32_bf16 %0, %1, %2, %0" : "+v"(c) : "v"(a), "v"(b)); }
template <class T> DEVI void pin(T& v) { asm volatile("" : "+v"(v)); }
DEVI void zero_pin(f32x4& v) { v = (f32x4){0, 0, 0, 0}; pin(v); }
DEVI void mfma_fence() { asm volatile("s_nop 7\n\ts_nop 7" ::: "memory"); }
DEVI unsigned pk_bf16(float lo, float hi) { bf16x2 v = {(__bf16)lo, (__bf16)hi}; return __builtin_bit_cast(unsigned, v); }
DEVI u32x2 pk4(f32x4 v) { return (u32x2){pk_bf16(v[0], v[1]), pk_bf16(v[2], v[3])}; }
DEVI float bf_lo(unsigned u) { return __uint_as_float(u << 16); }
DEVI float bf_hi(unsigned u) { return __uint_as_float(u & 0xffff0000u); }
DEVI float rbf(float x) { return (float)(__bf16)x; }
DEVI float red16_add(float x) { auto s = __builtin_amdgcn_permlane16_swap(__float_as_uint(x), __float_as_uint(x), false, false); return __uint_as_float(s[0]) + __uint_as_float(s[1]); }
DEVI float red32_add(float x) { auto s = __builtin_amdgcn_permlane32_swap(__float_as_uint(x), __float_as_uint(x), false, false); return __uint_as_float(s[0]) + __uint_as_float(s[1]); }
DEVI float red16_max(float x) { auto s = __builtin_amdgcn_permlane16_swap(__float_as_uint(x), __float_as_uint(x), false, false); return fmaxf(__uint_as_float(s[0]), __uint_as_float(s[1])); }
DEVI float red32_max(float x) { auto s = __builtin_amdgcn_permlane32_swap(__float_as_uint(x), __float_as_uint(x), false, false); return fmaxf(__uint_as_float(s[0]), __uint_as_float(s[1])); }
DEVI float ln_rstd(float var) { return 1.0f / rbf(sqrtf(rbf(rbf(var) + 1e-5f))); }

DEVI float wave_sum(float x) {
  x += __builtin_bit_cast(float, __builtin_amdgcn_update_dpp(0, __builtin_bit_cast(int, x), 0xB1, 0xf, 0xf, true));
  x += __builtin_bit_cast(float, __builtin_amdgcn_update_dpp(0, __builtin_bit_cast(int, x), 0x4E, 0xf, 0xf, true));
  x += __builtin_bit_cast(float, __builtin_amdgcn_update_dpp(0, __builtin_bit_cast(int, x), 0x141, 0xf, 0xf, true));
  x += __builtin_bit_cast(float, __builtin_amdgcn_update_dpp(0, __builtin_bit_cast(int, x), 0x140, 0xf, 0xf, true));
  return red32_add(red16_add(x));
}

constexpr int CNT_PF = 64 * 64;
__global__ __launch_bounds__(512) void k0_ln(const u16* __restrict__ x, u16* __restrict__ xn, float* __restrict__ xmean,
                                             const u16* __restrict__ wo, u16* __restrict__ wbar, const u16* __restrict__ wqkv,
                                             unsigned* __restrict__ cnt) {
  const int lane = threadIdx.x & 63, w = threadIdx.x >> 6, row = blockIdx.x * 8 + w;
  TLS(0, 0);
  const u32x4* src = (const u32x4*)(x + (size_t)row * D_);
  const u32x4 a = src[lane], b = src[lane + 64];
  u32x4 wa, wb;
  const bool dow = __builtin_amdgcn_readfirstlane(w) < 4;
  if (dow) { const u32x4* ws = (const u32x4*)(wo + (size_t)(blockIdx.x * 4 + w) * D_); wa = ws[lane]; wb = ws[lane + 64]; }
  unsigned pfv = 0;
  if (lane < 24) pfv = *(const unsigned*)(wqkv + (size_t)(row * 24 + lane) * 64);
  float v[16];
#pragma unroll
  for (int i = 0; i < 4; ++i) { v[2 * i] = bf_lo(a[i]); v[2 * i + 1] = bf_hi(a[i]); v[8 + 2 * i] = bf_lo(b[i]); v[8 + 2 * i + 1] = bf_hi(b[i]); }
  float s = 0.f;
#pragma unroll
  for (int i = 0; i < 16; ++i) s += v[i];
  const float mean = wave_sum(s) * (1.f / D_);
  float q = 0.f;
#pragma unroll
  for (int i = 0; i < 16; ++i) { const float d = v[i] - mean; q += d * d; }
  const float mb = rbf(mean), rs = ln_rstd(wave_sum(q) * (1.f / D_));
  u32x4 oa, ob;
#pragma unroll
  for (int i = 0; i < 4; ++i) {
    const unsigned da = pk_bf16(v[2 * i] - mb, v[2 * i + 1] - mb), db = pk_bf16(v[8 + 2 * i] - mb, v[8 + 2 * i + 1] - mb);
    oa[i] = pk_bf16(bf_lo(da) * rs, bf_hi(da) * rs);
    ob[i] = pk_bf16(bf_lo(db) * rs, bf_hi(db) * rs);
  }
  char* dst = (char*)xn + (size_t)(lane >> 3) * KT_ + row * 128 + (lane & 7) * 16;
  *(u32x4*)dst = oa; *(u32x4*)(dst + 8 * KT_) = ob;
  if (threadIdx.x < 80) cnt[threadIdx.x < 64 ? CNT_PF + blockIdx.x * 64 + threadIdx.x : blockIdx.x * 16 + (threadIdx.x - 64)] = 0u;
  if (lane == 0) xmean[row] = mean;
  if (dow) {
    float t = 0.f;
#pragma unroll
    for (int i = 0; i < 4; ++i) t += (bf_lo(wa[i]) + bf_hi(wa[i])) + (bf_lo(wb[i]) + bf_hi(wb[i]));
    t = wave_sum(t) * (1.f / D_);
    const float hi = rbf(t);
    if (lane < 2) wbar[lane * D_ + blockIdx.x * 4 + w] = (u16)(__float_as_uint(lane ? rbf(t - hi) : hi) >> 16);
  }
  asm volatile("" :: "v"(pfv));
  TLS(0, 3);
}

typedef __attribute__((address_space(1))) unsigned gu32;
DEVI void signal_panel(unsigned* add_to, int lane) {
  if (lane == 0) __hip_atomic_fetch_add((gu32*)add_to, 1u, __ATOMIC_RELAXED, __HIP_MEMORY_SCOPE_AGENT);
}
DEVI void wait_panel(unsigned* wait_on, unsigned target, unsigned* tmo, int lane) {
  for (unsigned spins = 0;; ++spins) {
    if ((unsigned)__builtin_amdgcn_readfirstlane(__hip_atomic_load((gu32*)wait_on, __ATOMIC_RELAXED, __HIP_MEMORY_SCOPE_AGENT)) >= target) break;
    if (spins > (1u << 15)) { if (lane == 0) __hip_atomic_store((gu32*)tmo, 1u, __ATOMIC_RELAXED, __HIP_MEMORY_SCOPE_AGENT); break; }
    __builtin_amdgcn_s_sleep(1);
  }
}
DEVI void acquire_agent() { asm volatile("" ::: "memory"); }

constexpr int K1_STAGE = 18 * 1024 + 24 * 1024;
constexpr int K1_NS = 3;
constexpr int K1_DUMMY = K1_STAGE * K1_NS;
constexpr int K1_EX = K1_STAGE, K1_OST = 2 * K1_STAGE;
constexpr int P2_W = K1_DUMMY + 1024, P2_NSW = 4;
constexpr int P2_WB = 122880;
constexpr int G_NSA = 4;
constexpr int K3_AH = 16384;
constexpr int K3_UOFF = 15360 + 16;
constexpr int K3_BB = K3_UOFF + 15360;
constexpr int K3_B0 = 4 * K3_AH;
constexpr int K3_TAB = K3_B0 + 3 * K3_BB;
constexpr int K3_DUMMY = K3_TAB + 1024;
constexpr int P4_W = 65536, P4_NSW = 5;
#define P4_D 4
#define P4_PUB 3
constexpr int KM_LDS = 163840;

struct K1Frag { bf16x8 x[4], xh; s16x4 w[3][2][2]; };
struct G64Frag { bf16x8 x[2][2], wb[2]; s16x4 w[4][2][2]; };
struct P4Frag { bf16x8 x[4][2]; s16x4 w[4][2][2]; };

template <int K, int WOFF, int NSW> struct G64Loader {
  i32x4 srdA, srdW; unsigned voffA, voffW, sA, sW, lb;
  DEVI void init(const u16* A, const u16* W, int r0, int c0, int lw, int lane, unsigned lds0) {
    srdA = make_srd(A, S_ * K * 2); srdW = make_srd(W, K * D_ * 2);
    const int lr = lane >> 3, lc = lane & 7, fW = ((lane >> 4) & 1) | ((lw & 1) << 1);
    voffA = (unsigned)((8 * lw + lr) * 128 + ((lc ^ ((4 * lw + (lane >> 4)) & 7)) << 4));
    voffW = (unsigned)((8 * lw + lr) * (D_ * 2) + (((((lc >> 1) ^ fW) << 1) | (lc & 1)) << 4));
    sA = (unsigned)r0 * 128; sW = (unsigned)c0 * 2; lb = lds0 + lw * 1024;
  }
  DEVI void issueW(int t, int slot) const {
#pragma unroll
    for (int j = 0; j < 2; ++j) dma16(lb + WOFF + slot * 8192 + j * 4096, voffW, srdW, sW + (unsigned)(t * 64 + 32 * j) * (D_ * 2));
  }
  DEVI void issueA(int t, int slot) const {
#pragma unroll
    for (int j = 0; j < 4; ++j) dma16_dev(lb + slot * 16384 + j * 4096, voffA, srdA, sA + j * 4096 + t * KT_);
  }
  template <class F> DEVI void run(F&& tail) const {
    constexpr int NT = K / 64;
    static_assert(NSW == 4 && G_NSA == 4, "");
    issueA(0, 0); issueA(1, 1);
    wait_vm<4>(); bar();
    issueA(2, 2); issueA(3, 3);
    wait_vm<8>(); bar(); issueW(4, 0); issueA(4, 0);
    wait_vm<10>(); bar(); issueW(5, 1); issueA(5, 1);
    for (int t = 2; t < NT - 4; ++t) { wait_vm<12>(); bar(); issueW(t + 4, t & 3); issueA(t + 4, t & 3); }
    wait_vm<12>(); bar(); tail(IC<0>{});
    wait_vm<9>(); bar(); tail(IC<1>{});
    wait_vm<6>(); bar(); tail(IC<2>{});
  }
};

template <int K, bool LN2, int WOFF, int NSW>
DEVI void g64_compute(LAS char* smem, int w, int lane, int r0, int c0, int tn, const u16* __restrict__ R, u16* __restrict__ O,
                      u16* __restrict__ OC, float* __restrict__ ssq, const float* __restrict__ xmean) {
  constexpr int NT = K / 64;
  const int g = lane >> 4, c = lane & 15;
  const int ecol = c0 + ((g & 1) << 4) + ((g >> 1) << 3);
  const int sw = (lane >> 1) & 7;
  const int aoff0 = (32 * w + c) * 128 + ((g ^ sw) << 4);
  const int q4 = c >> 2, p4 = lane & 3, fR = ((q4 >> 1) & 1) | ((g & 1) << 1);
  const int woff0 = WOFF + (8 * g + q4) * 128 + (fR << 5) + 8 * p4;
  const int wboff = P2_WB + (c & 1) * 2048 + g * 16;
  constexpr int NR = LN2 ? 22 : 20;
  auto rd = [&](G64Frag& f, auto t_c, auto r_c) {
    constexpr int t = decltype(t_c)::v, r = decltype(r_c)::v, ks = r / 10, q = r % 10;
    if constexpr (r >= 20) f.wb[r - 20] = lds_b128(smem + wboff + t * 128 + (r - 20) * 64);
    else if constexpr (q < 2) f.x[q][ks] = lds_b128(smem + (t % G_NSA) * 16384 + (aoff0 ^ (ks << 6)) + q * 2048);
    else f.w[(q - 2) >> 1][ks][q & 1] = lds_tr(smem + (t % NSW) * 8192 + (woff0 ^ (((q - 2) >> 1) << 5)) + ks * 4096 + (q & 1) * 512);
  };
  f32x4 acc[2][4], accm[2];
#pragma unroll
  for (int m = 0; m < 2; ++m) {
    if constexpr (LN2) zero_pin(accm[m]);
#pragma unroll
    for (int j = 0; j < 4; ++j) zero_pin(acc[m][j]);
  }
  constexpr int NM = LN2 ? 20 : 16;
  auto mm = [&](const G64Frag& f, auto i_c) {
    constexpr int i = decltype(i_c)::v, ks = i >> 3, j = (i >> 1) & 3, m = i & 1;
    if constexpr (i < 16) mfma_ip(acc[m][j], cat8(f.w[j][ks][0], f.w[j][ks][1]), f.x[m][ks]);
    else mfma_ip(accm[m], f.wb[j & 1], f.x[m][j & 1]);
  };
  G64Frag fr[2];
  u32x4 res[2][2]; float xm[2];
  bar();
  TLS(LN2 ? 2 : 4, 1);
  sfor<0, NR>([&](auto r) { rd(fr[0], IC<0>{}, r); });
  mfma_fence(); SB();
  sfor<0, NT>([&](auto t_c) {
    constexpr int t = decltype(t_c)::v;
    if constexpr (t < NT - 1) { wait_lgkm0(); bar(); SB(); }
    if constexpr (t == 4) {
#pragma unroll
      for (int m = 0; m < 2; ++m) {
#pragma unroll
        for (int jp = 0; jp < 2; ++jp) ld_async(res[m][jp], R + (size_t)(r0 + 32 * w + 16 * m + c) * D_ + ecol + 32 * jp);
        ld_async(xm[m], xmean + r0 + 32 * w + 16 * m + c);
      }
      SB();
    }
    sfor<0, NM>([&](auto i) {
      constexpr int ii = decltype(i)::v;
      if constexpr (t < NT - 1 && 2 * ii < NR) { rd(fr[(t + 1) & 1], IC<t + 1>{}, IC<2 * ii>{}); rd(fr[(t + 1) & 1], IC<t + 1>{}, IC<2 * ii + 1>{}); }
      mm(fr[t & 1], i);
      SB();
    });
  });
  mfma_fence();
  asm volatile("s_waitcnt vmcnt(0)" : "+v"(res[0][0]), "+v"(res[0][1]), "+v"(res[1][0]), "+v"(res[1][1]), "+v"(xm[0]), "+v"(xm[1]) :: "memory");
  SB();
  TLS(LN2 ? 2 : 4, 2);
  float qs[2];
  LAS char* st = smem + ((NT - 2) % G_NSA) * 16384 + w * 4096;
#pragma unroll
  for (int m = 0; m < 2; ++m) {
    const int row = r0 + 32 * w + 16 * m + c;
    float mean = 0.f, mb = 0.f, q = 0.f;
    if constexpr (LN2) { mean = xm[m] + (accm[m][0] + accm[m][1]); mb = rbf(mean); }
#pragma unroll
    for (int jp = 0; jp < 2; ++jp) {
      const u32x2 a0 = pk4(acc[m][2 * jp]), a1 = pk4(acc[m][2 * jp + 1]);
      auto e0 = __builtin_amdgcn_permlane16_swap(a0[0], a1[0], false, false);
      auto e1 = __builtin_amdgcn_permlane16_swap(a0[1], a1[1], false, false);
      const unsigned e[4] = {e0[0], e1[0], e0[1], e1[1]};
      u32x4 ov, oc;
#pragma unroll
      for (int i = 0; i < 4; ++i) {
        ov[i] = pk_bf16(bf_lo(e[i]) + bf_lo(res[m][jp][i]), bf_hi(e[i]) + bf_hi(res[m][jp][i]));
        if constexpr (LN2) {
          const float h0 = bf_lo(ov[i]), h1 = bf_hi(ov[i]);
          oc[i] = pk_bf16(h0 - mb, h1 - mb);
          q += (h0 - mean) * (h0 - mean) + (h1 - mean) * (h1 - mean);
        }
      }
      if constexpr (LN2) *(LAS u32x4*)(st + (16 * m + c) * 128 + (((4 * jp + 2 * (g & 1) + (g >> 1)) ^ (c & 7)) << 4)) = oc;
      *(u32x4*)(O + (size_t)row * D_ + ecol + 32 * jp) = ov;
    }
    if constexpr (LN2) {
      qs[m] = red32_add(red16_add(q));
    }
  }
  if constexpr (LN2) { if (lane < 32) st_wt(ssq + tn * S_ + r0 + 32 * w + lane, g ? qs[1] : qs[0]); }
  if constexpr (LN2) {
#pragma unroll
    for (int i = 0; i < 4; ++i) {
      const int rr = 8 * i + (lane >> 3);
      st_wt((char*)OC + (size_t)tn * KT_ + (r0 + 32 * w + rr) * 128 + (((lane ^ rr) & 7) << 4), *(const LAS u32x4*)(st + i * 1024 + lane * 16));
    }
  }
  TLS(LN2 ? 2 : 4, 3);
}

__global__ __launch_bounds__(512, 2) void km_block(const u16* __restrict__ x, const u16* __restrict__ wqkv, const u16* __restrict__ wo,
                                                   const u16* __restrict__ wg, const u16* __restrict__ wu, const u16* __restrict__ wd,
                                                   u16* __restrict__ out, char* __restrict__ ws) {
  __shared__ __attribute__((aligned(16))) char smem_[KM_LDS];
  LAS char* smem = (LAS char*)smem_;
  const unsigned lds0 = (unsigned)(size_t)smem;
  const int tid = threadIdx.x, lane = tid & 63, w = __builtin_amdgcn_readfirstlane(tid >> 6);
  const int bid = blockIdx.x, xcd = bid & 7, idx = bid >> 3;
  const int tm = 4 * (xcd >> 1) + (idx & 3), tn = 8 * (xcd & 1) + (idx >> 2);
  const int r0 = tm * 128;
  const int tm3 = 4 * (xcd >> 2) + (idx & 3), tn3 = 8 * (xcd & 3) + (idx >> 2);
  u16* const hc = (u16*)(ws + WS_HC); u16* const o = (u16*)(ws + WS_O); u16* const h = (u16*)(ws + WS_H);
  float* const ssq = (float*)(ws + WS_SSQ); const float* const xmean = (const float*)(ws + WS_XMEAN);
  const u16* const wbar = (const u16*)(ws + WS_WBAR); u16* const act = (u16*)(ws + WS_ACT); const u16* const xn = (const u16*)(ws + WS_XN);
  unsigned* const cnt = (unsigned*)(ws + WS_CNT);
  const int w4 = w & 3;
  const int lr = lane >> 3, lc = lane & 7;
  const int g = lane >> 4, c = lane & 15;
  TLS(1, 0);
  auto p3_voffB = [&](int j) {
    const int ch = (w4 + 4 * j) * 64 + lane, gr = ch / 120, r = ch - gr * 120, rr = r / 14, pos = r - rr * 14;
    return r < 112 ? (unsigned)((8 * gr + rr) * (F_ * 2) + (pos << 4)) : OOB;
  };
  const unsigned sB3 = (unsigned)tn3 * 224u;

  if (w >= 4) {
    const int lw = w4;
    {
      constexpr int NT = D_ / 64;
      const i32x4 srdA = make_srd(xn, S_ * D_ * 2), srdW = make_srd(wqkv, D_ * 3 * D_ * 2);
      const unsigned voffA = (unsigned)((8 * lw + lr) * 128 + ((lc ^ ((4 * lw + (lane >> 4)) & 7)) << 4));
      const int fW = ((lane >> 4) & 1) | ((lw & 1) << 1);
      const unsigned voffW = (unsigned)((8 * lw + lr) * (3 * D_ * 2) + (((((lc >> 1) ^ fW) << 1) | (lc & 1)) << 4));
      const unsigned sA = (unsigned)r0 * 128, sW = (unsigned)tn * 128;
      const bool halo = lw < 2;
      const unsigned sX = (unsigned)(r0 >= 16 ? r0 - 16 : 0) * 128;
      auto issue = [&](int t, int slot) {
        const unsigned lb = lds0 + slot * K1_STAGE + lw * 1024;
#pragma unroll
        for (int j = 0; j < 2; ++j) dma16(lb + 18432 + j * 4096, voffW, srdW, sW + (unsigned)(t * 64 + 32 * j) * (3 * D_ * 2));
#pragma unroll
        for (int j = 0; j < 4; ++j) dma16(lb + j * 4096, voffA, srdA, sA + j * 4096 + t * KT_);
#pragma unroll
        for (int s = 1; s < 3; ++s)
#pragma unroll
          for (int j = 0; j < 2; ++j) dma16(lb + 18432 + s * 8192 + j * 4096, voffW, srdW, sW + (unsigned)(t * 64 + 32 * j) * (3 * D_ * 2) + s * (D_ * 2));
        if (halo) dma16(lb + 16384, voffA, srdA, sX + t * KT_);
      };
      issue(0, 0); issue(1, 1);
      wait_vm<10>(); bar();
      issue(2, 2);
      int slot = 0;
      for (int t = 0; t < NT - 3; ++t) {
        wait_vm<10>(); bar();
        issue(t + 3, slot);
        slot = slot == 2 ? 0 : slot + 1;
      }
    }
    G64Loader<D_, P2_W, P2_NSW> L2;
    L2.init(o, wo, r0, tn * 64, lw, lane, lds0);
    wait_vm<10>(); bar();
#pragma unroll
    for (int s = 0; s < P2_NSW; ++s) L2.issueW(s, s);
    wait_vm<2 * P2_NSW>(); bar();
    dma16(lds0 + K1_DUMMY, lane < 56 ? (unsigned)lane * 128u : OOB, make_srd((lw & 1) ? wu : wg, D_ * F_ * 2), (unsigned)bid * 14336u + (lw >> 1) * 7168u);
    bar(); bar();
    dma16(lds0 + P2_WB + lw * 1024, (unsigned)lane * 16u, make_srd(wbar, 4096), (unsigned)lw * 1024u);
    bar();
    bar();
    acquire_agent();
    const unsigned vo3[4] = {p3_voffB(0), p3_voffB(1), p3_voffB(2), p3_voffB(3)};
    L2.run([&](auto i_c) {
      constexpr int i = decltype(i_c)::v;
      const i32x4 srd = make_srd(i == 1 ? wu : wg, D_ * F_ * 2);
      const unsigned lb = lds0 + K3_B0 + (i == 1 ? K3_UOFF : (i == 2 ? K3_BB : 0)) + lw * 1024, so = sB3 + (i == 2 ? 64 * F_ * 2 : 0);
#pragma unroll
      for (int j = 0; j < 3; ++j) dma16(lb + j * 4096, vo3[j], srd, so);
      if (lw < 3) dma16(lb + 3 * 4096, vo3[3], srd, so);
      if constexpr (i == 2)
        dma16(lds0 + K1_DUMMY, lane < 56 ? (unsigned)lane * 128u : OOB, make_srd((lw & 1) ? wu : wg, D_ * F_ * 2), 512u * F_ * 2 + (unsigned)bid * 14336u + (lw >> 1) * 7168u);
    });
  } else {
    constexpr int NT = D_ / 64;
    const int hh = w & 1, rh = w >> 1;
    const int sw = (lane >> 1) & 7;
    const int aoff0 = (64 * rh + c) * 128 + ((g ^ sw) << 4), hoff0 = (128 + c) * 128 + ((g ^ sw) << 4);
    const int q4 = c >> 2, p4 = lane & 3, fR = ((q4 >> 1) & 1) | ((g & 1) << 1);
    const int woff0 = 18432 + (8 * g + q4) * 128 + (((2 * hh + rh) ^ fR) << 5) + 8 * p4;
    auto rd = [&](K1Frag& f, int slot, int ks, auto r_c) {
      constexpr int r = decltype(r_c)::v;
      const LAS char* b = smem + slot * K1_STAGE;
      if constexpr (r >= 2 && r <= 5) f.x[r - 2] = lds_b128(b + (aoff0 ^ (ks << 6)) + (r - 2) * 2048);
      else if constexpr (r == 12) f.xh = lds_b128(b + (hoff0 ^ (ks << 6)));
      else {
        constexpr int rr = r < 2 ? r : (r < 12 ? r - 4 : r - 5);
        constexpr int mat = rr >> 2, sl = (rr >> 1) & 1, hf = rr & 1;
        f.w[mat][sl][hf] = lds_tr(b + (woff0 ^ (sl << 5)) + ks * 4096 + mat * 8192 + hf * 512);
      }
    };
    f32x4 aq[4][2], ak[4][2], av[4][2], ahk, ahv;
#pragma unroll
    for (int m = 0; m < 4; ++m)
#pragma unroll
      for (int s = 0; s < 2; ++s) { zero_pin(aq[m][s]); zero_pin(ak[m][s]); zero_pin(av[m][s]); }
    zero_pin(ahk); zero_pin(ahv);
    constexpr int NM = 26;
    auto mm = [&](const K1Frag& f, auto i_c) {
      constexpr int i = decltype(i_c)::v;
      if constexpr (i < 8) mfma_ip(aq[i & 3][i >> 2], cat8(f.w[0][i >> 2][0], f.w[0][i >> 2][1]), f.x[i & 3]);
      else if constexpr (i < 16) mfma_ip(ak[i & 3][(i - 8) >> 2], cat8(f.w[1][(i - 8) >> 2][0], f.w[1][(i - 8) >> 2][1]), f.x[i & 3]);
      else if constexpr (i == 16) mfma_ip(ahk, cat8(f.w[1][0][0], f.w[1][0][1]), f.xh);
      else if constexpr (i < 25) mfma_ip(av[(i - 17) & 3][(i - 17) >> 2], f.x[(i - 17) & 3], cat8(f.w[2][(i - 17) >> 2][0], f.w[2][(i - 17) >> 2][1]));
      else mfma_ip(ahv, f.xh, cat8(f.w[2][0][0], f.w[2][0][1]));
    };
    K1Frag fa, fb;
    __builtin_amdgcn_s_setprio(1);
    bar();
    TLS(1, 1);
    sfor<0, 17>([&](auto r) { rd(fa, 0, 0, r); });
    mfma_fence(); SB();
    constexpr int NB = 4;
    sfor<0, NT>([&](auto t_c) {
      constexpr int t = decltype(t_c)::v, slot = t % K1_NS;
      constexpr bool last = t == NT - 1;
      sfor<0, NM>([&](auto i) { mm(fa, i); if constexpr (decltype(i)::v < 17) rd(fb, slot, 1, i); SB(); });
      sfor<0, NB>([&](auto i) { mm(fb, i); SB(); });
      if constexpr (!last) { wait_lgkm0(); bar(); SB(); }
      sfor<NB, NM>([&](auto i) {
        mm(fb, i);
        if constexpr (!last && decltype(i)::v - NB < 17) rd(fa, (slot + 1) % K1_NS, 0, IC<decltype(i)::v - NB>{});
        SB();
      });
    });
    mfma_fence(); SB();
    TLS(1, 2);
    LAS char* ex = smem + K1_EX;
    auto exa = [&](int h_, int r_, int kind, int b) { return (LAS u32x2*)(ex + ((((h_ * 2 + r_) * 2 + kind) * 2 + b) * 64 + lane) * 8); };
    u32x2 qp[4][2], kp[4][2], vp[4][2];
#pragma unroll
    for (int s = 0; s < 2; ++s) { kp[3][s] = pk4(ak[3][s]); vp[3][s] = pk4(av[3][s]); }
    *exa(hh, 0, 0, rh) = pk4(ahk); *exa(hh, 0, 1, rh) = pk4(ahv);
    if (rh == 0) {
      *exa(hh, 1, 0, 0) = kp[3][0]; *exa(hh, 1, 0, 1) = kp[3][1];
      *exa(hh, 1, 1, 0) = vp[3][0]; *exa(hh, 1, 1, 1) = vp[3][1];
    }
#pragma unroll
    for (int m = 0; m < 4; ++m)
#pragma unroll
      for (int s = 0; s < 2; ++s) { qp[m][s] = pk4(aq[m][s]); if (m < 3) { kp[m][s] = pk4(ak[m][s]); vp[m][s] = pk4(av[m][s]); } }
    TLR(a);
    wait_lgkm0(); bar();
    TLR(b);
    u32x2 kprev[2], vprev[2];
#pragma unroll
    for (int s = 0; s < 2; ++s) { kprev[s] = *exa(hh, rh, 0, s ^ rh); vprev[s] = *exa(hh, rh, 1, s ^ rh); }
    const bool odd = c & 1;
    const int d1 = c - 4 * g - (c & 1);
    const float NINF = -__builtin_inff();
    const float b0a = d1 + 16 <= 14 ? 0.f : NINF, b0b = d1 + 14 <= 14 ? 0.f : NINF, b1a = d1 >= 0 ? 0.f : NINF, b1b = d1 - 2 >= 0 ? 0.f : NINF;
    const bool first = (r0 + 64 * rh) == 0;
    const unsigned pmask = odd ? 0xffff0000u : 0x0000ffffu;
    const bf16x8 ones = __builtin_bit_cast(bf16x8, (u32x4){0x3f803f80u, 0x3f803f80u, 0x3f803f80u, 0x3f803f80u});
    const float SL2E = 0.17677669529663687f * 1.4426950408889634f;
    const int och = 4 * hh + 2 * ((g & 1) ^ rh) + (g >> 1);
    LAS char* ost = smem + K1_OST + (64 * rh + c) * 128 + ((och ^ (c & 7)) << 4);
#pragma unroll
    for (int mi = 0; mi < 4; ++mi) {
      const int m = (mi + 1) & 3;
      const bf16x8 qf = cat8u(qp[m][0], qp[m][1]);
      const f32x4 s0 = mfma(m ? cat8u(kp[m - 1][0], kp[m - 1][1]) : cat8u(kprev[0], kprev[1]), qf, (f32x4){0, 0, 0, 0});
      const f32x4 s1 = mfma(cat8u(kp[m][0], kp[m][1]), qf, (f32x4){0, 0, 0, 0});
      const float l0 = fmaf(odd ? s0[1] : s0[0], SL2E, (m == 0 && first) ? NINF : b0a);
      const float l1 = fmaf(odd ? s0[3] : s0[2], SL2E, (m == 0 && first) ? NINF : b0b);
      const float l2 = fmaf(odd ? s1[1] : s1[0], SL2E, b1a);
      const float l3 = fmaf(odd ? s1[3] : s1[2], SL2E, b1b);
      const float mx = red32_max(red16_max(fmaxf(fmaxf(l0, l1), fmaxf(l2, l3))));
      const float p0 = __builtin_amdgcn_exp2f(l0 - mx), p1 = __builtin_amdgcn_exp2f(l1 - mx), p2 = __builtin_amdgcn_exp2f(l2 - mx), p3 = __builtin_amdgcn_exp2f(l3 - mx);
      const bf16x8 pf = __builtin_bit_cast(bf16x8, (u32x4){pk_bf16(p0, p0) & pmask, pk_bf16(p1, p1) & pmask, pk_bf16(p2, p2) & pmask, pk_bf16(p3, p3) & pmask});
      const f32x4 sum = mfma(ones, pf, (f32x4){0, 0, 0, 0});
      const f32x4 o0 = mfma(cat8u(m ? vp[m - 1][0] : vprev[0], vp[m][0]), pf, (f32x4){0, 0, 0, 0});
      const f32x4 o1 = mfma(cat8u(m ? vp[m - 1][1] : vprev[1], vp[m][1]), pf, (f32x4){0, 0, 0, 0});
      const float inv = __builtin_amdgcn_rcpf(sum[0]);
      const u32x2 op0 = pk4(o0 * inv), op1 = pk4(o1 * inv);
      auto e0 = __builtin_amdgcn_permlane16_swap(op0[0], op1[0], false, false);
      auto e1 = __builtin_amdgcn_permlane16_swap(op0[1], op1[1], false, false);
      *(LAS u32x4*)(ost + m * 2048) = (u32x4){e0[0], e1[0], e0[1], e1[1]};
    }
    wait_lgkm0();
    TLR(c);
    bar();
#pragma unroll
    for (int i = 0; i < 4; ++i) {
      const int rr = 32 * w + 8 * i + lr;
      st_wt((char*)o + (size_t)tn * KT_ + (r0 + rr) * 128 + (((lane ^ rr) & 7) << 4), *(const LAS u32x4*)(smem + K1_OST + (4 * w + i) * 1024 + lane * 16));
    }
    wait_lgkm0();
    TLR(d);
    wait_vm<0>();
    TLS(1, 3);
    TLW(1);
    bar();
    if (w == 0) { signal_panel(cnt + tm * 64, lane); wait_panel(cnt + tm * 64, 16, cnt + 32 * 64, lane); }
    bar();
    TLS(2, 0);
    g64_compute<D_, true, P2_W, P2_NSW>(smem, w, lane, r0, tn * 64, tn, x, h, hc, ssq, xmean);
    wait_lgkm0(); wait_vm<0>();
    __builtin_amdgcn_s_setprio(0);
  }
  bar();
  if (w == 0) signal_panel(cnt + (16 + (tm >> 1)) * 64, lane);
  {
    const int grp = w >> 2, wm = (w >> 1) & 1, wn = w & 1;
    const int r3 = tm3 * 256;
    const i32x4 srdA = make_srd(hc, S_ * D_ * 2), srdB = make_srd(grp ? wu : wg, D_ * F_ * 2);
    const unsigned voffA = (unsigned)((8 * w4 + lr) * 128 + ((lc ^ ((4 * w4 + (lane >> 4)) & 7)) << 4));
    const unsigned sA = (unsigned)r3 * 128;
    auto dmaA = [&](int t, int half, int buf, int j) {
      dma16_dev(lds0 + (half * 2 + buf) * K3_AH + w4 * 1024 + j * 4096, voffA, srdA, sA + half * 16384 + j * 4096 + t * KT_);
    };
    unsigned voffB[4];
#pragma unroll
    for (int j = 0; j < 4; ++j) voffB[j] = p3_voffB(j);
    auto dmaB = [&](int t, int buf, int j) {
      if (j < 3 || w4 < 3) dma16(lds0 + K3_B0 + buf * K3_BB + grp * K3_UOFF + w4 * 1024 + j * 4096, voffB[j], srdB, sB3 + (unsigned)(t * 64) * (F_ * 2));
    };
    const int sw = (lane >> 1) & 7;
    const int aoff0 = grp * 2 * K3_AH + (64 * wm + c) * 128 + ((g ^ sw) << 4);
    const int q4 = c >> 2, p4 = lane & 3;
    const int boffP = K3_B0 + g * 1920 + q4 * 224 + wn * 112 + 8 * p4;
    const int boffM = K3_B0 + g * 1920 + q4 * 224 + wn * 112 + 96 + (p4 >> 1) * K3_UOFF + 8 * (p4 & 1);
    bf16x8 fx[4][2]; s16x4 fw[7][2][2];
    auto rd = [&](int abuf, int bbuf, auto r_c) {
      constexpr int r = decltype(r_c)::v, ks = r / 18, q = r % 18;
      if constexpr (q < 14) {
        constexpr int j = q >> 1, hf = q & 1;
        fw[j][ks][hf] = lds_tr(smem + (j == 6 ? boffM : boffP) + bbuf * K3_BB + (j < 3 ? j * 32 : (j < 6 ? K3_UOFF + (j - 3) * 32 : 0)) + ks * 7680 + hf * 896);
      } else fx[q - 14][ks] = lds_b128(smem + (aoff0 ^ (ks << 6)) + abuf * K3_AH + (q - 14) * 2048);
    };
    f32x4 acc[4][7];
#pragma unroll
    for (int m = 0; m < 4; ++m)
#pragma unroll
      for (int j = 0; j < 7; ++j) zero_pin(acc[m][j]);
    auto mma = [&]() {
      __builtin_amdgcn_s_setprio(1);
#pragma unroll
      for (int ks = 0; ks < 2; ++ks)
#pragma unroll
        for (int j = 0; j < 7; ++j)
#pragma unroll
          for (int m = 0; m < 4; ++m) mfma_ip(acc[m][j], cat8(fw[j][ks][0], fw[j][ks][1]), fx[m][ks]);
      __builtin_amdgcn_s_setprio(0);
    };
    constexpr int NT = D_ / 64;
#pragma unroll
    for (int j = 0; j < 4; ++j) pin(voffB[j]);
    if (w == 0) wait_panel(cnt + (16 + tm3) * 64, 32, cnt + 32 * 64, lane);
    bar();
    acquire_agent();
    TLS(3, 0);
#pragma unroll
    for (int j = 0; j < 4; ++j) dmaA(0, grp, 0, j);
    if (grp == 0) {
      wait_vm<0>(); bar();
    } else {
      wait_vm<4>(); bar();
      float st[16];
      const float* sp = ssq + r3 + (tid & 255);
#pragma unroll
      for (int i = 0; i < 16; ++i) asm volatile("global_load_dword %0, %1, off sc1" : "=v"(st[i]) : "v"(sp + i * S_));
#pragma unroll
      for (int j = 0; j < 4; ++j) { dmaA(1, 0, 1, j); dmaB(1, 1, j); }
      asm volatile("s_waitcnt vmcnt(7)" : "+v"(st[0]), "+v"(st[1]), "+v"(st[2]), "+v"(st[3]), "+v"(st[4]), "+v"(st[5]), "+v"(st[6]), "+v"(st[7]),
                   "+v"(st[8]), "+v"(st[9]), "+v"(st[10]), "+v"(st[11]), "+v"(st[12]), "+v"(st[13]), "+v"(st[14]), "+v"(st[15]) :: "memory");
      float q = 0.f;
#pragma unroll
      for (int i = 0; i < 16; ++i) q += st[i];
      *(LAS float*)(smem + K3_TAB + (tid & 255) * 4) = ln_rstd(q * (1.f / D_));
      wait_lgkm0(); bar();
    }
    mfma_fence(); SB();
    const int ahalf = grp ^ 1;
    sfor<0, NT>([&](auto t_c) {
      constexpr int t = decltype(t_c)::v, ab = t & 1, bb = t % 3;
      const int abuf = ab ^ ahalf;
      sfor<0, 36>([&](auto r) {
        constexpr int rr = decltype(r)::v;
        rd(ab, bb, r);
        if constexpr (t < NT - 2 && (rr & 3) == 3 && rr < 32) {
          constexpr int d = rr >> 2;
          if constexpr (d & 1) dmaB(t + 2, (bb + 2) % 3, d >> 1); else dmaA(t + 1 + grp, ahalf, abuf, d >> 1);
        }
        if constexpr (t == NT - 2 && (rr & 7) == 3 && rr < 32) { if (grp == 0) dmaA(t + 1, 1, ab ^ 1, rr >> 3); }
        if constexpr (t < NT - 2) SB();
      });
      if constexpr (t == NT - 2)
        dma16(lds0 + K3_DUMMY, lane < 28 ? (unsigned)lane * 128u : OOB, make_srd(wd, D_ * F_ * 2), (unsigned)bid * 28672u + w * 3584u);
      wait_lgkm0();
      if constexpr (t < NT - 2) wait_vm<7>();
      else if constexpr (t == NT - 2) { if (grp == 0) wait_vm<5>(); else wait_vm<1>(); }
      else wait_vm<1>();
      bar(); SB();
      mma();
      SB();
      if constexpr (t < NT - 1) bar(); else { if (grp == 0) bar(); }
      SB();
    });
    mfma_fence(); SB();
    TLS(3, 2);
    const float C0 = -2.0f * 1.4426950408889634f * 0.7978845608028654f, C1 = C0 * 0.044715f;
    auto geglu2 = [&](f32x2 gt, f32x2 up, float c0a, float c1a, float ia2) {
      const f32x2 z = gt * (gt * gt * c1a + c0a);
      f32x2 e; e.x = __builtin_amdgcn_exp2f(z.x); e.y = __builtin_amdgcn_exp2f(z.y);
      const f32x2 d = e * ia2 + ia2;
      f32x2 r; r.x = __builtin_amdgcn_rcpf(d.x); r.y = __builtin_amdgcn_rcpf(d.y);
      const f32x2 o2 = gt * up * r;
      return pk_bf16(o2.x, o2.y);
    };
    const int rowb = 128 * grp + 64 * wm;
    LAS char* ast = smem + w * 8192 + c * 128 + 8 * (g & 1);
    const int csw = c & 7;
#pragma unroll
    for (int mp = 0; mp < 2; ++mp) {
      const int mrow = 2 * mp + (g >> 1);
      const float a = *(const LAS float*)(smem + K3_TAB + (rowb + 16 * mrow + c) * 4);
      const float c0a = C0 * a, c1a = C1 * a * a * a, ia2 = __builtin_amdgcn_rcpf(a * a);
      float gt[4], up[4];
#pragma unroll
      for (int r = 0; r < 4; ++r) {
        auto s = __builtin_amdgcn_permlane32_swap(__float_as_uint(acc[2 * mp][6][r]), __float_as_uint(acc[2 * mp + 1][6][r]), false, false);
        gt[r] = __uint_as_float(s[0]); up[r] = __uint_as_float(s[1]);
      }
      *(LAS u32x2*)(ast + mrow * 2048 + ((6 ^ csw) << 4)) =
          (u32x2){geglu2((f32x2){gt[0], gt[1]}, (f32x2){up[0], up[1]}, c0a, c1a, ia2), geglu2((f32x2){gt[2], gt[3]}, (f32x2){up[2], up[3]}, c0a, c1a, ia2)};
      *(LAS u32x2*)(ast + mrow * 2048 + ((7 ^ csw) << 4)) = (u32x2){0u, 0u};
    }
    char* actw = (char*)act + (size_t)(2 * tn3 + wn) * KT_ + (r3 + rowb + lr) * 128 + ((lc ^ lr) << 4);
#pragma unroll
    for (int m = 0; m < 4; ++m) {
      const float a = *(const LAS float*)(smem + K3_TAB + (rowb + 16 * m + c) * 4);
      const float c0a = C0 * a, c1a = C1 * a * a * a, ia2 = __builtin_amdgcn_rcpf(a * a);
#pragma unroll
      for (int b = 0; b < 3; ++b) {
        const f32x4 gt = acc[m][b], up = acc[m][3 + b];
        *(LAS u32x2*)(ast + m * 2048 + (((2 * b + (g >> 1)) ^ csw) << 4)) =
            (u32x2){geglu2((f32x2){gt[0], gt[1]}, (f32x2){up[0], up[1]}, c0a, c1a, ia2), geglu2((f32x2){gt[2], gt[3]}, (f32x2){up[2], up[3]}, c0a, c1a, ia2)};
      }
#pragma unroll
      for (int i = 0; i < 2; ++i) st_wt(actw + (2 * m + i) * 1024, *(const LAS u32x4*)(smem + w * 8192 + (2 * m + i) * 1024 + lane * 16));
    }
    TLS(3, 3);
  }
  {
    constexpr int NX = 32 - P4_D, NS4 = 32 + P4_D;
    const int kh = tn & 1;
    const i32x4 srdW = make_srd(wd, F_ * D_ * 2);
    const int fW = ((lane >> 4) & 1) | ((w4 & 1) << 1);
    const unsigned voffW0 = (unsigned)((8 * w4 + lr) * (D_ * 2) + (((((lc >> 1) ^ fW) << 1) | (lc & 1)) << 4));
    const bool w7 = w4 != 3;
    const unsigned stx = kh ? (unsigned)NS4 : 0u;
    auto issueW = [&](auto s_c) {
      constexpr int s = decltype(s_c)::v;
      const unsigned T = s < NX ? s + stx : s, lb = lds0 + P4_W + (s % P4_NSW) * 16384 + w4 * 1024, so = T * (56 * D_ * 2);
      dma16(lb, voffW0, srdW, so + tn * 128);
      if constexpr (s < NX) dma16(lb + 8192, voffW0, srdW, so + (tn ^ 1) * 128);
      if (w7) {
        dma16(lb + 4096, voffW0, srdW, so + 32 * D_ * 2 + tn * 128);
        if constexpr (s < NX) dma16(lb + 12288, voffW0, srdW, so + 32 * D_ * 2 + (tn ^ 1) * 128);
      }
    };
    wait_lgkm0(); wait_vm<0>();
    if (w < 4) {
      sfor<0, P4_NSW>([&](auto s) { issueW(s); });
      for (int i = w; i < 2 * P4_NSW; i += 4) *(LAS u32x4*)(smem + P4_W + i * 8192 + 7168 + lane * 16) = (u32x4){0u, 0u, 0u, 0u};
      wait_lgkm0();
    }
    bar();
    if (w == 4) { signal_panel(cnt + (24 + tm3) * 64, lane); wait_panel(cnt + (24 + (tm >> 1)) * 64, 32, cnt + 32 * 64, lane); }
    bar();
    TLS(4, 0);
    if (w >= 4) {
      acquire_agent();
      const i32x4 srdA = make_srd(act, 64 * KT_);
      const unsigned voffA = (unsigned)((8 * w4 + lr) * 128 + ((lc ^ ((4 * w4 + (lane >> 4)) & 7)) << 4)), sA = (unsigned)r0 * 128;
      auto issueA = [&](auto s_c) {
        constexpr int s = decltype(s_c)::v;
        const unsigned T = s < NX ? s + stx : s;
#pragma unroll
        for (int j = 0; j < 4; ++j) dma16_dev(lds0 + (s % G_NSA) * 16384 + w4 * 1024 + j * 4096, voffA, srdA, sA + j * 4096 + T * KT_);
      };
      issueA(IC<0>{}); issueA(IC<1>{});
      wait_vm<4>(); bar();
      issueA(IC<2>{}); issueA(IC<3>{});
      sfor<0, NS4 - 1>([&](auto s_c) {
        constexpr int s = decltype(s_c)::v;
        constexpr auto nw = [](int st) { return (st < 0 || st + P4_NSW >= NS4) ? 0 : (st + P4_NSW < NX ? 2 : 1); };
        constexpr int n = nw(s - 2) + nw(s - 1) + (s + 2 < NS4 ? 4 : 0) + (s + 3 < NS4 ? 4 : 0);
        wait_vm<n>(); bar();
        if constexpr (s + P4_NSW < NS4) issueW(IC<s + P4_NSW>{});
        if constexpr (s + 4 < NS4) issueA(IC<s + 4>{});
      });
    } else {
      __builtin_amdgcn_s_setprio(1);
      const int wm = w >> 1, wo_ = w & 1;
      const int sw = (lane >> 1) & 7;
      const int aoff0 = (64 * wm + c) * 128 + ((g ^ sw) << 4);
      const int q4 = c >> 2, p4 = lane & 3, fR = ((q4 >> 1) & 1) | ((g & 1) << 1);
      const int woff0 = P4_W + wo_ * 8192 + (8 * g + q4) * 128 + (fR << 5) + 8 * p4;
      auto rd = [&](P4Frag& f, auto s_c, auto r_c) {
        constexpr int s = decltype(s_c)::v, r = decltype(r_c)::v, ks = r / 12, q = r % 12;
        if constexpr (q < 4) f.x[q][ks] = lds_b128(smem + (s % G_NSA) * 16384 + (aoff0 ^ (ks << 6)) + q * 2048);
        else f.w[(q - 4) >> 1][ks][q & 1] = lds_tr(smem + (s % P4_NSW) * 16384 + (woff0 ^ (((q - 4) >> 1) << 5)) + ks * 4096 + (q & 1) * 512);
      };
      f32x4 acc[4][4];
#pragma unroll
      for (int m = 0; m < 4; ++m)
#pragma unroll
        for (int j = 0; j < 4; ++j) zero_pin(acc[m][j]);
      P4Frag fr[2];
      auto step = [&](auto s_c, auto pre_c) {
        constexpr int s = decltype(s_c)::v;
        sfor<0, 32>([&](auto i_c) {
          constexpr int i = decltype(i_c)::v, ks = i >> 4, j = (i >> 2) & 3, m = i & 3;
          if constexpr (decltype(pre_c)::v && i < 24) rd(fr[(s + 1) & 1], IC<s + 1>{}, i_c);
          mfma_ip(acc[m][j], cat8(fr[s & 1].w[j][ks][0], fr[s & 1].w[j][ks][1]), fr[s & 1].x[m][ks]);
          SB();
        });
      };
      wait_vm<0>();
      bar();
      TLS(4, 1);
      sfor<0, 24>([&](auto r) { rd(fr[0], IC<0>{}, r); });
      mfma_fence(); SB();
      sfor<0, NX>([&](auto s) { wait_lgkm0(); bar(); SB(); step(s, IC<1>{}); });
      f32x4* const part = (f32x4*)(ws + WS_PART);
      mfma_fence(); SB();
      if (wo_) {
#pragma unroll
        for (int i = 0; i < 16; ++i) st_wt(part + ((bid * 2 + wm) * 16 + i) * 64 + lane, acc[i >> 2][i & 3]);
        sfor<NX, NS4 - 1>([&](auto s_c) {
          if constexpr (decltype(s_c)::v == NX + P4_PUB) {
            wait_vm<0>();
            if (lane == 0) __hip_atomic_store((gu32*)(cnt + CNT_PF + (bid * 2 + wm) * 32), 1u, __ATOMIC_RELAXED, __HIP_MEMORY_SCOPE_AGENT);
          }
          bar();
        });
      } else {
        sfor<NX, NS4 - 1>([&](auto s) { wait_lgkm0(); bar(); SB(); step(s, IC<1>{}); });
        const int pb = bid ^ 32;
        unsigned* const pf = cnt + CNT_PF + (pb * 2 + wm) * 32;
        unsigned fl;
        ld_async_dev(fl, pf);
        const int ecol = tn * 64 + ((g & 1) << 4) + ((g >> 1) << 3);
        u32x4 res[4][2];
#pragma unroll
        for (int m = 0; m < 4; ++m)
#pragma unroll
          for (int jp = 0; jp < 2; ++jp) ld_async(res[m][jp], h + (size_t)(r0 + 64 * wm + 16 * m + c) * D_ + ecol + 32 * jp);
        SB();
        step(IC<NS4 - 1>{}, IC<0>{});
        mfma_fence();
        asm volatile("s_waitcnt vmcnt(8)" : "+v"(fl) :: "memory");
        TLS(4, 2);
        if (__builtin_amdgcn_readfirstlane(fl) == 0u) {
          for (unsigned spins = 0;; ++spins) {
            if (__builtin_amdgcn_readfirstlane(__hip_atomic_load((gu32*)pf, __ATOMIC_RELAXED, __HIP_MEMORY_SCOPE_AGENT)) != 0u) break;
            if (spins > (1u << 15)) { if (lane == 0) __hip_atomic_store((gu32*)(cnt + 32 * 64), 1u, __ATOMIC_RELAXED, __HIP_MEMORY_SCOPE_AGENT); break; }
            __builtin_amdgcn_s_sleep(1);
          }
        }
        f32x4 pp[16];
#pragma unroll
        for (int i = 0; i < 16; ++i) ld_async_dev(pp[i], part + ((pb * 2 + wm) * 16 + i) * 64 + lane);
        asm volatile("s_waitcnt vmcnt(0)" : "+v"(pp[0]), "+v"(pp[1]), "+v"(pp[2]), "+v"(pp[3]), "+v"(pp[4]), "+v"(pp[5]), "+v"(pp[6]), "+v"(pp[7]),
                     "+v"(pp[8]), "+v"(pp[9]), "+v"(pp[10]), "+v"(pp[11]), "+v"(pp[12]), "+v"(pp[13]), "+v"(pp[14]), "+v"(pp[15]),
                     "+v"(res[0][0]), "+v"(res[0][1]), "+v"(res[1][0]), "+v"(res[1][1]), "+v"(res[2][0]), "+v"(res[2][1]), "+v"(res[3][0]), "+v"(res[3][1]) :: "memory");
#pragma unroll
        for (int m = 0; m < 4; ++m)
#pragma unroll
          for (int jp = 0; jp < 2; ++jp) {
            const u32x2 a0 = pk4(acc[m][2 * jp] + pp[4 * m + 2 * jp]), a1 = pk4(acc[m][2 * jp + 1] + pp[4 * m + 2 * jp + 1]);
            auto e0 = __builtin_amdgcn_permlane16_swap(a0[0], a1[0], false, false);
            auto e1 = __builtin_amdgcn_permlane16_swap(a0[1], a1[1], false, false);
            const unsigned e[4] = {e0[0], e1[0], e0[1], e1[1]};
            u32x4 ov;
#pragma unroll
            for (int i = 0; i < 4; ++i) ov[i] = pk_bf16(bf_lo(e[i]) + bf_lo(res[m][jp][i]), bf_hi(e[i]) + bf_hi(res[m][jp][i]));
            *(u32x4*)(out + (size_t)(r0 + 64 * wm + 16 * m + c) * D_ + ecol + 32 * jp) = ov;
          }
        TLS(4, 3);
      }
    }
  }
}

extern "C" void kernel_launch(void* const* d_in, const int* in_sizes, int n_in, void* d_out, int out_size, void* d_ws, size_t ws_size,
                              hipStream_t stream) {
  const u16* x = (const u16*)d_in[0];
  const u16* wqkv = (const u16*)d_in[1];
  const u16* wo = (const u16*)d_in[2];
  const u16* wg = (const u16*)d_in[4];
  const u16* wu = (const u16*)d_in[5];
  const u16* wd = (const u16*)d_in[6];
  char* ws = (char*)d_ws;
  k0_ln<<<256, 512, 0, stream>>>(x, (u16*)(ws + WS_XN), (float*)(ws + WS_XMEAN), wo, (u16*)(ws + WS_WBAR), wqkv, (unsigned*)(ws + WS_CNT));
  km_block<<<256, 512, 0, stream>>>(x, wqkv, wo, wg, wu, wd, (u16*)d_out, ws);
}
```

```cpp
#include <hip/hip_runtime.h>
#include <hip/hip_bf16.h>

typedef unsigned short u16;
typedef __attribute__((ext_vector_type(8))) __bf16 bf16x8;
typedef __attribute__((ext_vector_type(2))) __bf16 bf16x2;
typedef __attribute__((ext_vector_type(4))) short s16x4;
typedef __attribute__((ext_vector_type(4))) float f32x4;
typedef __attribute__((ext_vector_type(2))) float f32x2;
typedef __attribute__((ext_vector_type(4))) int i32x4;
typedef __attribute__((ext_vector_type(4))) unsigned u32x4;
typedef __attribute__((ext_vector_type(2))) unsigned u32x2;
#define LAS __attribute__((address_space(3)))
#define DEVI __device__ __forceinline__

constexpr int S_ = 2048, D_ = 1024, F_ = 3584;
constexpr size_t WS_HC = 0, WS_O = 4u << 20, WS_H = 8u << 20, WS_SSQ = 12u << 20, WS_XMEAN = 13u << 20, WS_WBAR = 14u << 20, WS_CNT = 15u << 20,
                 WS_ACT = 16u << 20, WS_XN = 32u << 20, WS_PART = 40u << 20;
constexpr unsigned KT_ = S_ * 128;
constexpr unsigned OOB = 0x80000000u;

DEVI void wait_lgkm0() { asm volatile("s_waitcnt lgkmcnt(0)" ::: "memory"); }
template <int N> struct IC { static constexpr int v = N; };
template <int I, int N, class F> DEVI void sfor(F&& f) { if constexpr (I < N) { f(IC<I>{}); sfor<I + 1, N>(f); } }
#define TLR(n) do {} while (0)
#define TLW(k) do {} while (0)
#define TLS(k, i) do {} while (0)

DEVI i32x4 make_srd(const void* p, unsigned bytes) {
  unsigned long long a = (unsigned long long)p;
  i32x4 r;
  r.x = (int)(unsigned)a; r.y = (int)((unsigned)(a >> 32) & 0xffffu); r.z = (int)bytes; r.w = 0x00020000;
  return r;
}
DEVI void dma16(unsigned lds_addr, unsigned voff, i32x4 srd, unsigned soff) {
  asm volatile("s_mov_b32 m0, %0\n\ts_nop 0\n\tbuffer_load_dwordx4 %1, %2, %3 offen lds"
               :: "s"(lds_addr), "v"(voff), "s"(srd), "s"(soff));
}
DEVI void touch_lines(unsigned& sink, unsigned voff, i32x4 srd, unsigned soff) {
  asm volatile("buffer_load_dword %0, %1, %2, %3 offen" : "+v"(sink) : "v"(voff), "s"(srd), "s"(soff));
}
DEVI void dma16_dev(unsigned lds_addr, unsigned voff, i32x4 srd, unsigned soff) {
  asm volatile("s_mov_b32 m0, %0\n\ts_nop 0\n\tbuffer_load_dwordx4 %1, %2, %3 offen sc1 lds"
               :: "s"(lds_addr), "v"(voff), "s"(srd), "s"(soff));
}
template <int N> DEVI void wait_vm() { asm volatile("s_waitcnt vmcnt(%0)" :: "n"(N) : "memory"); }
DEVI void bar() { asm volatile("" ::: "memory"); __builtin_amdgcn_s_barrier(); asm volatile("" ::: "memory"); }
#define SB() __builtin_amdgcn_sched_barrier(0)

DEVI void st_wt(void* p, u32x4 v) { asm volatile("global_store_dwordx4 %0, %1, off sc1\n\ts_nop 1" :: "v"(p), "v"(v) : "memory"); }
DEVI void st_wt(void* p, f32x4 v) { asm volatile("global_store_dwordx4 %0, %1, off sc1\n\ts_nop 1" :: "v"(p), "v"(v) : "memory"); }
DEVI void ld_async(u32x4& d, const void* p) { asm volatile("global_load_dwordx4 %0, %1, off" : "=v"(d) : "v"(p)); }
DEVI void ld_async(float& d, const void* p) { asm volatile("global_load_dword %0, %1, off" : "=v"(d) : "v"(p)); }
DEVI void ld_async_dev(f32x4& d, const void* p) { asm volatile("global_load_dwordx4 %0, %1, off sc1" : "=v"(d) : "v"(p)); }
DEVI void ld_async_dev(unsigned& d, const void* p) { asm volatile("global_load_dword %0, %1, off sc1" : "=v"(d) : "v"(p)); }
DEVI void st_wt(void* p, float v) { asm volatile("global_store_dword %0, %1, off sc1" :: "v"(p), "v"(v) : "memory"); }
DEVI bf16x8 lds_b128(const LAS char* p) { return *(const LAS bf16x8*)p; }
DEVI s16x4 lds_tr(const LAS char* p) { return __builtin_amdgcn_ds_read_tr16_b64_v4i16((LAS s16x4*)p); }
DEVI bf16x8 cat8(s16x4 a, s16x4 b) { return __builtin_bit_cast(bf16x8, __builtin_shufflevector(a, b, 0, 1, 2, 3, 4, 5, 6, 7)); }
DEVI bf16x8 cat8u(u32x2 a, u32x2 b) { return __builtin_bit_cast(bf16x8, (u32x4){a[0], a[1], b[0], b[1]}); }
DEVI f32x4 mfma(bf16x8 a, bf16x8 b, f32x4 c) { return __builtin_amdgcn_mfma_f32_16x16x32_bf16(a, b, c, 0, 0, 0); }
DEVI void mfma_ip(f32x4& c, bf16x8 a, bf16x8 b) { asm volatile("v_mfma_f32_16x16x32_bf16 %0, %1, %2, %0" : "+v"(c) : "v"(a), "v"(b)); }
template <class T> DEVI void pin(T& v) { asm volatile("" : "+v"(v)); }
DEVI void zero_pin(f32x4& v) { v = (f32x4){0, 0, 0, 0}; pin(v); }
DEVI void mfma_fence() { asm volatile("s_nop 7\n\ts_nop 7" ::: "memory"); }
DEVI unsigned pk_bf16(float lo, float hi) { bf16x2 v = {(__bf16)lo, (__bf16)hi}; return __builtin_bit_cast(unsigned, v); }
DEVI u32x2 pk4(f32x4 v) { return (u32x2){pk_bf16(v[0], v[1]), pk_bf16(v[2], v[3])}; }
DEVI float bf_lo(unsigned u) { return __uint_as_float(u << 16); }
DEVI float bf_hi(unsigned u) { return __uint_as_float(u & 0xffff0000u); }
DEVI float rbf(float x) { return (float)(__bf16)x; }
DEVI float red16_add(float x) { auto s = __builtin_amdgcn_permlane16_swap(__float_as_uint(x), __float_as_uint(x), false, false); return __uint_as_float(s[0]) + __uint_as_float(s[1]); }
DEVI float red32_add(float x) { auto s = __builtin_amdgcn_permlane32_swap(__float_as_uint(x), __float_as_uint(x), false, false); return __uint_as_float(s[0]) + __uint_as_float(s[1]); }
DEVI float red16_max(float x) { auto s = __builtin_amdgcn_permlane16_swap(__float_as_uint(x), __float_as_uint(x), false, false); return fmaxf(__uint_as_float(s[0]), __uint_as_float(s[1])); }
DEVI float red32_max(float x) { auto s = __builtin_amdgcn_permlane32_swap(__float_as_uint(x), __float_as_uint(x), false, false); return fmaxf(__uint_as_float(s[0]), __uint_as_float(s[1])); }
DEVI float ln_rstd(float var) { return 1.0f / rbf(sqrtf(rbf(rbf(var) + 1e-5f))); }

DEVI float wave_sum(float x) {
  x += __builtin_bit_cast(float, __builtin_amdgcn_update_dpp(0, __builtin_bit_cast(int, x), 0xB1, 0xf, 0xf, true));
  x += __builtin_bit_cast(float, __builtin_amdgcn_update_dpp(0, __builtin_bit_cast(int, x), 0x4E, 0xf, 0xf, true));
  x += __builtin_bit_cast(float, __builtin_amdgcn_update_dpp(0, __builtin_bit_cast(int, x), 0x141, 0xf, 0xf, true));
  x += __builtin_bit_cast(float, __builtin_amdgcn_update_dpp(0, __builtin_bit_cast(int, x), 0x140, 0xf, 0xf, true));
  return red32_add(red16_add(x));
}

constexpr int CNT_PF = 64 * 64;
__global__ __launch_bounds__(512) void k0_ln(const u16* __restrict__ x, u16* __restrict__ xn, float* __restrict__ xmean,
                                             const u16* __restrict__ wo, u16* __restrict__ wbar, const u16* __restrict__ wqkv,
                                             unsigned* __restrict__ cnt) {
  const int lane = threadIdx.x & 63, w = threadIdx.x >> 6, row = blockIdx.x * 8 + w;
  TLS(0, 0);
  const u32x4* src = (const u32x4*)(x + (size_t)row * D_);
  const u32x4 a = src[lane], b = src[lane + 64];
  u32x4 wa, wb;
  const bool dow = __builtin_amdgcn_readfirstlane(w) < 4;
  if (dow) { const u32x4* ws = (const u32x4*)(wo + (size_t)(blockIdx.x * 4 + w) * D_); wa = ws[lane]; wb = ws[lane + 64]; }
  unsigned pfv = 0;
  if (lane < 24) pfv = *(const unsigned*)(wqkv + (size_t)(row * 24 + lane) * 64);
  float v[16];
#pragma unroll
  for (int i = 0; i < 4; ++i) { v[2 * i] = bf_lo(a[i]); v[2 * i + 1] = bf_hi(a[i]); v[8 + 2 * i] = bf_lo(b[i]); v[8 + 2 * i + 1] = bf_hi(b[i]); }
  float s = 0.f;
#pragma unroll
  for (int i = 0; i < 16; ++i) s += v[i];
  const float mean = wave_sum(s) * (1.f / D_);
  float q = 0.f;
#pragma unroll
  for (int i = 0; i < 16; ++i) { const float d = v[i] - mean; q += d * d; }
  const float mb = rbf(mean), rs = ln_rstd(wave_sum(q) * (1.f / D_));
  u32x4 oa, ob;
#pragma unroll
  for (int i = 0; i < 4; ++i) {
    const unsigned da = pk_bf16(v[2 * i] - mb, v[2 * i + 1] - mb), db = pk_bf16(v[8 + 2 * i] - mb, v[8 + 2 * i + 1] - mb);
    oa[i] = pk_bf16(bf_lo(da) * rs, bf_hi(da) * rs);
    ob[i] = pk_bf16(bf_lo(db) * rs, bf_hi(db) * rs);
  }
  char* dst = (char*)xn + (size_t)(lane >> 3) * KT_ + row * 128 + (lane & 7) * 16;
  *(u32x4*)dst = oa; *(u32x4*)(dst + 8 * KT_) = ob;
  if (threadIdx.x < 80) cnt[threadIdx.x < 64 ? CNT_PF + blockIdx.x * 64 + threadIdx.x : blockIdx.x * 16 + (threadIdx.x - 64)] = 0u;
  if (lane == 0) xmean[row] = mean;
  if (dow) {
    float t = 0.f;
#pragma unroll
    for (int i = 0; i < 4; ++i) t += (bf_lo(wa[i]) + bf_hi(wa[i])) + (bf_lo(wb[i]) + bf_hi(wb[i]));
    t = wave_sum(t) * (1.f / D_);
    const float hi = rbf(t);
    if (lane < 2) wbar[lane * D_ + blockIdx.x * 4 + w] = (u16)(__float_as_uint(lane ? rbf(t - hi) : hi) >> 16);
  }
  asm volatile("" :: "v"(pfv));
  TLS(0, 3);
}

typedef __attribute__((address_space(1))) unsigned gu32;
DEVI void signal_panel(unsigned* add_to, int lane) {
  if (lane == 0) __hip_atomic_fetch_add((gu32*)add_to, 1u, __ATOMIC_RELAXED, __HIP_MEMORY_SCOPE_AGENT);
}
DEVI void wait_panel(unsigned* wait_on, unsigned target, unsigned* tmo, int lane) {
  for (unsigned spins = 0;; ++spins) {
    if ((unsigned)__builtin_amdgcn_readfirstlane(__hip_atomic_load((gu32*)wait_on, __ATOMIC_RELAXED, __HIP_MEMORY_SCOPE_AGENT)) >= target) break;
    if (spins > (1u << 15)) { if (lane == 0) __hip_atomic_store((gu32*)tmo, 1u, __ATOMIC_RELAXED, __HIP_MEMORY_SCOPE_AGENT); break; }
    __builtin_amdgcn_s_sleep(1);
  }
}
DEVI void acquire_agent() { asm volatile("" ::: "memory"); }

DEVI unsigned kord(int t, int n, int c) {
  const int v = t + 3 - 2 * ((t - c) & 3);
  return (unsigned)(v >= 0 && v < n ? v : t);
}
#define ROT 2
DEVI unsigned kord1(int t, int n, int c) { return (ROT & 1) ? kord(t, n, c) : (unsigned)t; }
DEVI unsigned kord2(int t, int n, int c) { return (ROT & 2) ? kord(t, n, c) : (unsigned)t; }
DEVI unsigned kord4(int t, int n, int c) { return (ROT & 4) ? kord(t, n, c) : (unsigned)t; }
constexpr int K1_STAGE = 18 * 1024 + 24 * 1024;
constexpr int K1_NS = 3;
constexpr int K1_DUMMY = K1_STAGE * K1_NS;
constexpr int K1_EX = K1_STAGE, K1_OST = 2 * K1_STAGE;
constexpr int P2_W = K1_DUMMY + 1024, P2_NSW = 4;
constexpr int P2_WB = 122880;
constexpr int G_NSA = 4;
constexpr int K3_AH = 16384;
constexpr int K3_UOFF = 15360 + 16;
constexpr int K3_BB = K3_UOFF + 15360;
constexpr int K3_B0 = 4 * K3_AH;
constexpr int K3_TAB = K3_B0 + 3 * K3_BB;
constexpr int K3_DUMMY = K3_TAB + 1024;
constexpr int P4_W = 65536, P4_NSW = 5;
#define P4_D 4
#define P4_PUB 3
constexpr int KM_LDS = 163840;

struct K1Frag { bf16x8 x[4], xh; s16x4 w[3][2][2]; };
struct G64Frag { bf16x8 x[2][2], wb[2]; s16x4 w[4][2][2]; };
struct P4Frag { bf16x8 x[4][2]; s16x4 w[4][2][2]; };

template <int K, int WOFF, int NSW> struct G64Loader {
  i32x4 srdA, srdW; unsigned voffA, voffW, sA, sW, lb; int cls;
  DEVI void init(const u16* A, const u16* W, int r0, int c0, int lw, int lane, unsigned lds0, int cls_) {
    cls = cls_;
    srdA = make_srd(A, S_ * K * 2); srdW = make_srd(W, K * D_ * 2);
    const int lr = lane >> 3, lc = lane & 7, fW = ((lane >> 4) & 1) | ((lw & 1) << 1);
    voffA = (unsigned)((8 * lw + lr) * 128 + ((lc ^ ((4 * lw + (lane >> 4)) & 7)) << 4));
    voffW = (unsigned)((8 * lw + lr) * (D_ * 2) + (((((lc >> 1) ^ fW) << 1) | (lc & 1)) << 4));
    sA = (unsigned)r0 * 128; sW = (unsigned)c0 * 2; lb = lds0 + lw * 1024;
  }
  DEVI void issueW(int t_, int slot) const {
    const unsigned t = kord2(t_, K / 64, cls);
#pragma unroll
    for (int j = 0; j < 2; ++j) dma16(lb + WOFF + slot * 8192 + j * 4096, voffW, srdW, sW + (unsigned)(t * 64 + 32 * j) * (D_ * 2));
  }
  DEVI void issueA(int t_, int slot) const {
    const unsigned t = kord2(t_, K / 64, cls);
#pragma unroll
    for (int j = 0; j < 4; ++j) dma16_dev(lb + slot * 16384 + j * 4096, voffA, srdA, sA + j * 4096 + t * KT_);
  }
  template <class F> DEVI void run(F&& tail) const {
    constexpr int NT = K / 64;
    static_assert(NSW == 4 && G_NSA == 4, "");
    issueA(0, 0); issueA(1, 1);
    wait_vm<4>(); bar();
    issueA(2, 2); issueA(3, 3);
    wait_vm<8>(); bar(); issueW(4, 0); issueA(4, 0);
    wait_vm<10>(); bar(); issueW(5, 1); issueA(5, 1);
    for (int t = 2; t < NT - 4; ++t) { wait_vm<12>(); bar(); issueW(t + 4, t & 3); issueA(t + 4, t & 3); }
    wait_vm<12>(); bar(); tail(IC<0>{});
    wait_vm<9>(); bar(); tail(IC<1>{});
    wait_vm<6>(); bar(); tail(IC<2>{});
  }
};

template <int K, bool LN2, int WOFF, int NSW>
DEVI void g64_compute(LAS char* smem, int w, int lane, int r0, int c0, int tn, const u16* __restrict__ R, u16* __restrict__ O,
                      u16* __restrict__ OC, float* __restrict__ ssq, const float* __restrict__ xmean, int cls) {
  constexpr int NT = K / 64;
  const int g = lane >> 4, c = lane & 15;
  const int ecol = c0 + ((g & 1) << 4) + ((g >> 1) << 3);
  const int sw = (lane >> 1) & 7;
  const int aoff0 = (32 * w + c) * 128 + ((g ^ sw) << 4);
  const int q4 = c >> 2, p4 = lane & 3, fR = ((q4 >> 1) & 1) | ((g & 1) << 1);
  const int woff0 = WOFF + (8 * g + q4) * 128 + (fR << 5) + 8 * p4;
  const int wboff = P2_WB + (c & 1) * 2048 + g * 16;
  constexpr int NR = LN2 ? 22 : 20;
  auto rd = [&](G64Frag& f, auto t_c, auto r_c) {
    constexpr int t = decltype(t_c)::v, r = decltype(r_c)::v, ks = r / 10, q = r % 10;
    if constexpr (r >= 20) f.wb[r - 20] = lds_b128(smem + wboff + kord2(t, NT, cls) * 128 + (r - 20) * 64);
    else if constexpr (q < 2) f.x[q][ks] = lds_b128(smem + (t % G_NSA) * 16384 + (aoff0 ^ (ks << 6)) + q * 2048);
    else f.w[(q - 2) >> 1][ks][q & 1] = lds_tr(smem + (t % NSW) * 8192 + (woff0 ^ (((q - 2) >> 1) << 5)) + ks * 4096 + (q & 1) * 512);
  };
  f32x4 acc[2][4], accm[2];
#pragma unroll
  for (int m = 0; m < 2; ++m) {
    if constexpr (LN2) zero_pin(accm[m]);
#pragma unroll
    for (int j = 0; j < 4; ++j) zero_pin(acc[m][j]);
  }
  constexpr int NM = LN2 ? 20 : 16;
  auto mm = [&](const G64Frag& f, auto i_c) {
    constexpr int i = decltype(i_c)::v, ks = i >> 3, j = (i >> 1) & 3, m = i & 1;
    if constexpr (i < 16) mfma_ip(acc[m][j], cat8(f.w[j][ks][0], f.w[j][ks][1]), f.x[m][ks]);
    else mfma_ip(accm[m], f.wb[j & 1], f.x[m][j & 1]);
  };
  G64Frag fr[2];
  u32x4 res[2][2]; float xm[2];
  bar();
  TLS(LN2 ? 2 : 4, 1);
  sfor<0, NR>([&](auto r) { rd(fr[0], IC<0>{}, r); });
  mfma_fence(); SB();
  sfor<0, NT>([&](auto t_c) {
    constexpr int t = decltype(t_c)::v;
    if constexpr (t < NT - 1) { wait_lgkm0(); bar(); SB(); }
    if constexpr (t == 4) {
#pragma unroll
      for (int m = 0; m < 2; ++m) {
#pragma unroll
        for (int jp = 0; jp < 2; ++jp) ld_async(res[m][jp], R + (size_t)(r0 + 32 * w + 16 * m + c) * D_ + ecol + 32 * jp);
        ld_async(xm[m], xmean + r0 + 32 * w + 16 * m + c);
      }
      SB();
    }
    sfor<0, NM>([&](auto i) {
      constexpr int ii = decltype(i)::v;
      if constexpr (t < NT - 1 && 2 * ii < NR) { rd(fr[(t + 1) & 1], IC<t + 1>{}, IC<2 * ii>{}); rd(fr[(t + 1) & 1], IC<t + 1>{}, IC<2 * ii + 1>{}); }
      mm(fr[t & 1], i);
      SB();
    });
  });
  mfma_fence();
  asm volatile("s_waitcnt vmcnt(0)" : "+v"(res[0][0]), "+v"(res[0][1]), "+v"(res[1][0]), "+v"(res[1][1]), "+v"(xm[0]), "+v"(xm[1]) :: "memory");
  SB();
  TLS(LN2 ? 2 : 4, 2);
  float qs[2];
  LAS char* st = smem + ((NT - 2) % G_NSA) * 16384 + w * 4096;
#pragma unroll
  for (int m = 0; m < 2; ++m) {
    const int row = r0 + 32 * w + 16 * m + c;
    float mean = 0.f, mb = 0.f, q = 0.f;
    if constexpr (LN2) { mean = xm[m] + (accm[m][0] + accm[m][1]); mb = rbf(mean); }
#pragma unroll
    for (int jp = 0; jp < 2; ++jp) {
      const u32x2 a0 = pk4(acc[m][2 * jp]), a1 = pk4(acc[m][2 * jp + 1]);
      auto e0 = __builtin_amdgcn_permlane16_swap(a0[0], a1[0], false, false);
      auto e1 = __builtin_amdgcn_permlane16_swap(a0[1], a1[1], false, false);
      const unsigned e[4] = {e0[0], e1[0], e0[1], e1[1]};
      u32x4 ov, oc;
#pragma unroll
      for (int i = 0; i < 4; ++i) {
        ov[i] = pk_bf16(bf_lo(e[i]) + bf_lo(res[m][jp][i]), bf_hi(e[i]) + bf_hi(res[m][jp][i]));
        if constexpr (LN2) {
          const float h0 = bf_lo(ov[i]), h1 = bf_hi(ov[i]);
          oc[i] = pk_bf16(h0 - mb, h1 - mb);
          q += (h0 - mean) * (h0 - mean) + (h1 - mean) * (h1 - mean);
        }
      }
      if constexpr (LN2) *(LAS u32x4*)(st + (16 * m + c) * 128 + (((4 * jp + 2 * (g & 1) + (g >> 1)) ^ (c & 7)) << 4)) = oc;
      *(u32x4*)(O + (size_t)row * D_ + ecol + 32 * jp) = ov;
    }
    if constexpr (LN2) {
      qs[m] = red32_add(red16_add(q));
    }
  }
  if constexpr (LN2) { if (lane < 32) st_wt(ssq + tn * S_ + r0 + 32 * w + lane, g ? qs[1] : qs[0]); }
  if constexpr (LN2) {
#pragma unroll
    for (int i = 0; i < 4; ++i) {
      const int rr = 8 * i + (lane >> 3);
      st_wt((char*)OC + (size_t)tn * KT_ + (r0 + 32 * w + rr) * 128 + (((lane ^ rr) & 7) << 4), *(const LAS u32x4*)(st + i * 1024 + lane * 16));
    }
  }
  TLS(LN2 ? 2 : 4, 3);
}

__global__ __launch_bounds__(512, 2) void km_block(const u16* __restrict__ x, const u16* __restrict__ wqkv, const u16* __restrict__ wo,
                                                   const u16* __restrict__ wg, const u16* __restrict__ wu, const u16* __restrict__ wd,
                                                   u16* __restrict__ out, char* __restrict__ ws) {
  __shared__ __attribute__((aligned(16))) char smem_[KM_LDS];
  LAS char* smem = (LAS char*)smem_;
  const unsigned lds0 = (unsigned)(size_t)smem;
  const int tid = threadIdx.x, lane = tid & 63, w = __builtin_amdgcn_readfirstlane(tid >> 6);
  const int bid = blockIdx.x, xcd = bid & 7, idx = bid >> 3;
  const int tm = 4 * (xcd >> 1) + (idx & 3), tn = 2 * (idx >> 2) + (xcd & 1);
  const int cls = (idx + (idx >> 2)) & 3;
  const int r0 = tm * 128;
  const int tm3 = 4 * (xcd >> 2) + (idx & 3), tn3 = 8 * (xcd & 3) + (idx >> 2);
  u16* const hc = (u16*)(ws + WS_HC); u16* const o = (u16*)(ws + WS_O); u16* const h = (u16*)(ws + WS_H);
  float* const ssq = (float*)(ws + WS_SSQ); const float* const xmean = (const float*)(ws + WS_XMEAN);
  const u16* const wbar = (const u16*)(ws + WS_WBAR); u16* const act = (u16*)(ws + WS_ACT); const u16* const xn = (const u16*)(ws + WS_XN);
  unsigned* const cnt = (unsigned*)(ws + WS_CNT);
  const int w4 = w & 3;
  const int lr = lane >> 3, lc = lane & 7;
  const int g = lane >> 4, c = lane & 15;
  TLS(1, 0);
  auto p3_voffB = [&](int j) {
    const int ch = (w4 + 4 * j) * 64 + lane, gr = ch / 120, r = ch - gr * 120, rr = r / 14, pos = r - rr * 14;
    return r < 112 ? (unsigned)((8 * gr + rr) * (F_ * 2) + (pos << 4)) : OOB;
  };
  const unsigned sB3 = (unsigned)tn3 * 224u;

  if (w >= 4) {
    const int lw = w4;
    {
      constexpr int NT = D_ / 64;
      const i32x4 srdA = make_srd(xn, S_ * D_ * 2), srdW = make_srd(wqkv, D_ * 3 * D_ * 2);
      const unsigned voffA = (unsigned)((8 * lw + lr) * 128 + ((lc ^ ((4 * lw + (lane >> 4)) & 7)) << 4));
      const int fW = ((lane >> 4) & 1) | ((lw & 1) << 1);
      const unsigned voffW = (unsigned)((8 * lw + lr) * (3 * D_ * 2) + (((((lc >> 1) ^ fW) << 1) | (lc & 1)) << 4));
      const unsigned sA = (unsigned)r0 * 128, sW = (unsigned)tn * 128;
      const bool halo = lw < 2;
      const unsigned sX = (unsigned)(r0 >= 16 ? r0 - 16 : 0) * 128;
      auto issue = [&](int t_, int slot) {
        const unsigned t = kord1(t_, NT, cls);
        const unsigned lb = lds0 + slot * K1_STAGE + lw * 1024;
#pragma unroll
        for (int j = 0; j < 2; ++j) dma16(lb + 18432 + j * 4096, voffW, srdW, sW + (unsigned)(t * 64 + 32 * j) * (3 * D_ * 2));
#pragma unroll
        for (int j = 0; j < 4; ++j) dma16(lb + j * 4096, voffA, srdA, sA + j * 4096 + t * KT_);
#pragma unroll
        for (int s = 1; s < 3; ++s)
#pragma unroll
          for (int j = 0; j < 2; ++j) dma16(lb + 18432 + s * 8192 + j * 4096, voffW, srdW, sW + (unsigned)(t * 64 + 32 * j) * (3 * D_ * 2) + s * (D_ * 2));
        if (halo) dma16(lb + 16384, voffA, srdA, sX + t * KT_);
      };
      issue(0, 0); issue(1, 1);
      wait_vm<10>(); bar();
      issue(2, 2);
      int slot = 0;
      for (int t = 0; t < NT - 3; ++t) {
        wait_vm<10>(); bar();
        issue(t + 3, slot);
        slot = slot == 2 ? 0 : slot + 1;
      }
    }
    G64Loader<D_, P2_W, P2_NSW> L2;
    L2.init(o, wo, r0, tn * 64, lw, lane, lds0, cls);
    wait_vm<10>(); bar();
#pragma unroll
    for (int s = 0; s < P2_NSW; ++s) L2.issueW(s, s);
    wait_vm<2 * P2_NSW>(); bar();
    dma16(lds0 + K1_DUMMY, lane < 56 ? (unsigned)lane * 128u : OOB, make_srd((lw & 1) ? wu : wg, D_ * F_ * 2), (unsigned)bid * 14336u + (lw >> 1) * 7168u);
    bar(); bar();
    dma16(lds0 + P2_WB + lw * 1024, (unsigned)lane * 16u, make_srd(wbar, 4096), (unsigned)lw * 1024u);
    bar();
    bar();
    acquire_agent();
    const unsigned vo3[4] = {p3_voffB(0), p3_voffB(1), p3_voffB(2), p3_voffB(3)};
    L2.run([&](auto i_c) {
      constexpr int i = decltype(i_c)::v;
      const i32x4 srd = make_srd(i == 1 ? wu : wg, D_ * F_ * 2);
      const unsigned lb = lds0 + K3_B0 + (i == 1 ? K3_UOFF : (i == 2 ? K3_BB : 0)) + lw * 1024, so = sB3 + (i == 2 ? 64 * F_ * 2 : 0);
#pragma unroll
      for (int j = 0; j < 3; ++j) dma16(lb + j * 4096, vo3[j], srd, so);
      if (lw < 3) dma16(lb + 3 * 4096, vo3[3], srd, so);
      if constexpr (i == 2)
        dma16(lds0 + K1_DUMMY, lane < 56 ? (unsigned)lane * 128u : OOB, make_srd((lw & 1) ? wu : wg, D_ * F_ * 2), 512u * F_ * 2 + (unsigned)bid * 14336u + (lw >> 1) * 7168u);
    });
  } else {
    constexpr int NT = D_ / 64;
    const int hh = w & 1, rh = w >> 1;
    const int sw = (lane >> 1) & 7;
    const int aoff0 = (64 * rh + c) * 128 + ((g ^ sw) << 4), hoff0 = (128 + c) * 128 + ((g ^ sw) << 4);
    const int q4 = c >> 2, p4 = lane & 3, fR = ((q4 >> 1) & 1) | ((g & 1) << 1);
    const int woff0 = 18432 + (8 * g + q4) * 128 + (((2 * hh + rh) ^ fR) << 5) + 8 * p4;
    auto rd = [&](K1Frag& f, int slot, int ks, auto r_c) {
      constexpr int r = decltype(r_c)::v;
      const LAS char* b = smem + slot * K1_STAGE;
      if constexpr (r >= 2 && r <= 5) f.x[r - 2] = lds_b128(b + (aoff0 ^ (ks << 6)) + (r - 2) * 2048);
      else if constexpr (r == 12) f.xh = lds_b128(b + (hoff0 ^ (ks << 6)));
      else {
        constexpr int rr = r < 2 ? r : (r < 12 ? r - 4 : r - 5);
        constexpr int mat = rr >> 2, sl = (rr >> 1) & 1, hf = rr & 1;
        f.w[mat][sl][hf] = lds_tr(b + (woff0 ^ (sl << 5)) + ks * 4096 + mat * 8192 + hf * 512);
      }
    };
    f32x4 aq[4][2], ak[4][2], av[4][2], ahk, ahv;
#pragma unroll
    for (int m = 0; m < 4; ++m)
#pragma unroll
      for (int s = 0; s < 2; ++s) { zero_pin(aq[m][s]); zero_pin(ak[m][s]); zero_pin(av[m][s]); }
    zero_pin(ahk); zero_pin(ahv);
    constexpr int NM = 26;
    auto mm = [&](const K1Frag& f, auto i_c) {
      constexpr int i = decltype(i_c)::v;
      if constexpr (i < 8) mfma_ip(aq[i & 3][i >> 2], cat8(f.w[0][i >> 2][0], f.w[0][i >> 2][1]), f.x[i & 3]);
      else if constexpr (i < 16) mfma_ip(ak[i & 3][(i - 8) >> 2], cat8(f.w[1][(i - 8) >> 2][0], f.w[1][(i - 8) >> 2][1]), f.x[i & 3]);
      else if constexpr (i == 16) mfma_ip(ahk, cat8(f.w[1][0][0], f.w[1][0][1]), f.xh);
      else if constexpr (i < 25) mfma_ip(av[(i - 17) & 3][(i - 17) >> 2], f.x[(i - 17) & 3], cat8(f.w[2][(i - 17) >> 2][0], f.w[2][(i - 17) >> 2][1]));
      else mfma_ip(ahv, f.xh, cat8(f.w[2][0][0], f.w[2][0][1]));
    };
    K1Frag fa, fb;
    __builtin_amdgcn_s_setprio(1);
    bar();
    TLS(1, 1);
    sfor<0, 17>([&](auto r) { rd(fa, 0, 0, r); });
    mfma_fence(); SB();
    constexpr int NB = 4;
    sfor<0, NT>([&](auto t_c) {
      constexpr int t = decltype(t_c)::v, slot = t % K1_NS;
      constexpr bool last = t == NT - 1;
      sfor<0, NM>([&](auto i) { mm(fa, i); if constexpr (decltype(i)::v < 17) rd(fb, slot, 1, i); SB(); });
      sfor<0, NB>([&](auto i) { mm(fb, i); SB(); });
      if constexpr (!last) { wait_lgkm0(); bar(); SB(); }
      sfor<NB, NM>([&](auto i) {
        mm(fb, i);
        if constexpr (!last && decltype(i)::v - NB < 17) rd(fa, (slot + 1) % K1_NS, 0, IC<decltype(i)::v - NB>{});
        SB();
      });
    });
    mfma_fence(); SB();
    TLS(1, 2);
    LAS char* ex = smem + K1_EX;
    auto exa = [&](int h_, int r_, int kind, int b) { return (LAS u32x2*)(ex + ((((h_ * 2 + r_) * 2 + kind) * 2 + b) * 64 + lane) * 8); };
    u32x2 qp[4][2], kp[4][2], vp[4][2];
#pragma unroll
    for (int s = 0; s < 2; ++s) { kp[3][s] = pk4(ak[3][s]); vp[3][s] = pk4(av[3][s]); }
    *exa(hh, 0, 0, rh) = pk4(ahk); *exa(hh, 0, 1, rh) = pk4(ahv);
    if (rh == 0) {
      *exa(hh, 1, 0, 0) = kp[3][0]; *exa(hh, 1, 0, 1) = kp[3][1];
      *exa(hh, 1, 1, 0) = vp[3][0]; *exa(hh, 1, 1, 1) = vp[3][1];
    }
#pragma unroll
    for (int m = 0; m < 4; ++m)
#pragma unroll
      for (int s = 0; s < 2; ++s) { qp[m][s] = pk4(aq[m][s]); if (m < 3) { kp[m][s] = pk4(ak[m][s]); vp[m][s] = pk4(av[m][s]); } }
    TLR(a);
    wait_lgkm0(); bar();
    TLR(b);
    u32x2 kprev[2], vprev[2];
#pragma unroll
    for (int s = 0; s < 2; ++s) { kprev[s] = *exa(hh, rh, 0, s ^ rh); vprev[s] = *exa(hh, rh, 1, s ^ rh); }
    const bool odd = c & 1;
    const int d1 = c - 4 * g - (c & 1);
    const float NINF = -__builtin_inff();
    const float b0a = d1 + 16 <= 14 ? 0.f : NINF, b0b = d1 + 14 <= 14 ? 0.f : NINF, b1a = d1 >= 0 ? 0.f : NINF, b1b = d1 - 2 >= 0 ? 0.f : NINF;
    const bool first = (r0 + 64 * rh) == 0;
    const unsigned pmask = odd ? 0xffff0000u : 0x0000ffffu;
    const bf16x8 ones = __builtin_bit_cast(bf16x8, (u32x4){0x3f803f80u, 0x3f803f80u, 0x3f803f80u, 0x3f803f80u});
    const float SL2E = 0.17677669529663687f * 1.4426950408889634f;
    const int och = 4 * hh + 2 * ((g & 1) ^ rh) + (g >> 1);
    LAS char* ost = smem + K1_OST + (64 * rh + c) * 128 + ((och ^ (c & 7)) << 4);
#pragma unroll
    for (int mi = 0; mi < 4; ++mi) {
      const int m = (mi + 1) & 3;
      const bf16x8 qf = cat8u(qp[m][0], qp[m][1]);
      const f32x4 s0 = mfma(m ? cat8u(kp[m - 1][0], kp[m - 1][1]) : cat8u(kprev[0], kprev[1]), qf, (f32x4){0, 0, 0, 0});
      const f32x4 s1 = mfma(cat8u(kp[m][0], kp[m][1]), qf, (f32x4){0, 0, 0, 0});
      const float l0 = fmaf(odd ? s0[1] : s0[0], SL2E, (m == 0 && first) ? NINF : b0a);
      const float l1 = fmaf(odd ? s0[3] : s0[2], SL2E, (m == 0 && first) ? NINF : b0b);
      const float l2 = fmaf(odd ? s1[1] : s1[0], SL2E, b1a);
      const float l3 = fmaf(odd ? s1[3] : s1[2], SL2E, b1b);
      const float mx = red32_max(red16_max(fmaxf(fmaxf(l0, l1), fmaxf(l2, l3))));
      const float p0 = __builtin_amdgcn_exp2f(l0 - mx), p1 = __builtin_amdgcn_exp2f(l1 - mx), p2 = __builtin_amdgcn_exp2f(l2 - mx), p3 = __builtin_amdgcn_exp2f(l3 - mx);
      const bf16x8 pf = __builtin_bit_cast(bf16x8, (u32x4){pk_bf16(p0, p0) & pmask, pk_bf16(p1, p1) & pmask, pk_bf16(p2, p2) & pmask, pk_bf16(p3, p3) & pmask});
      const f32x4 sum = mfma(ones, pf, (f32x4){0, 0, 0, 0});
      const f32x4 o0 = mfma(cat8u(m ? vp[m - 1][0] : vprev[0], vp[m][0]), pf, (f32x4){0, 0, 0, 0});
      const f32x4 o1 = mfma(cat8u(m ? vp[m - 1][1] : vprev[1], vp[m][1]), pf, (f32x4){0, 0, 0, 0});
      const float inv = __builtin_amdgcn_rcpf(sum[0]);
      const u32x2 op0 = pk4(o0 * inv), op1 = pk4(o1 * inv);
      auto e0 = __builtin_amdgcn_permlane16_swap(op0[0], op1[0], false, false);
      auto e1 = __builtin_amdgcn_permlane16_swap(op0[1], op1[1], false, false);
      *(LAS u32x4*)(ost + m * 2048) = (u32x4){e0[0], e1[0], e0[1], e1[1]};
    }
    wait_lgkm0();
    TLR(c);
    bar();
#pragma unroll
    for (int i = 0; i < 4; ++i) {
      const int rr = 32 * w + 8 * i + lr;
      st_wt((char*)o + (size_t)tn * KT_ + (r0 + rr) * 128 + (((lane ^ rr) & 7) << 4), *(const LAS u32x4*)(smem + K1_OST + (4 * w + i) * 1024 + lane * 16));
    }
    wait_lgkm0();
    TLR(d);
    wait_vm<0>();
    TLS(1, 3);
    TLW(1);
    bar();
    if (w == 0) { signal_panel(cnt + tm * 64, lane); wait_panel(cnt + tm * 64, 16, cnt + 32 * 64, lane); }
    bar();
    TLS(2, 0);
    g64_compute<D_, true, P2_W, P2_NSW>(smem, w, lane, r0, tn * 64, tn, x, h, hc, ssq, xmean, cls);
    wait_lgkm0(); wait_vm<0>();
    __builtin_amdgcn_s_setprio(0);
  }
  bar();
  if (w == 0) signal_panel(cnt + (16 + (tm >> 1)) * 64, lane);
  {
    const int grp = w >> 2, wm = (w >> 1) & 1, wn = w & 1;
    const int r3 = tm3 * 256;
    const i32x4 srdA = make_srd(hc, S_ * D_ * 2), srdB = make_srd(grp ? wu : wg, D_ * F_ * 2);
    const unsigned voffA = (unsigned)((8 * w4 + lr) * 128 + ((lc ^ ((4 * w4 + (lane >> 4)) & 7)) << 4));
    const unsigned sA = (unsigned)r3 * 128;
    auto dmaA = [&](int t, int half, int buf, int j) {
      dma16_dev(lds0 + (half * 2 + buf) * K3_AH + w4 * 1024 + j * 4096, voffA, srdA, sA + half * 16384 + j * 4096 + t * KT_);
    };
    unsigned voffB[4];
#pragma unroll
    for (int j = 0; j < 4; ++j) voffB[j] = p3_voffB(j);
    auto dmaB = [&](int t, int buf, int j) {
      if (j < 3 || w4 < 3) dma16(lds0 + K3_B0 + buf * K3_BB + grp * K3_UOFF + w4 * 1024 + j * 4096, voffB[j], srdB, sB3 + (unsigned)(t * 64) * (F_ * 2));
    };
    const int sw = (lane >> 1) & 7;
    const int aoff0 = grp * 2 * K3_AH + (64 * wm + c) * 128 + ((g ^ sw) << 4);
    const int q4 = c >> 2, p4 = lane & 3;
    const int boffP = K3_B0 + g * 1920 + q4 * 224 + wn * 112 + 8 * p4;
    const int boffM = K3_B0 + g * 1920 + q4 * 224 + wn * 112 + 96 + (p4 >> 1) * K3_UOFF + 8 * (p4 & 1);
    bf16x8 fx[4][2]; s16x4 fw[7][2][2];
    auto rd = [&](int abuf, int bbuf, auto r_c) {
      constexpr int r = decltype(r_c)::v, ks = r / 18, q = r % 18;
      if constexpr (q < 14) {
        constexpr int j = q >> 1, hf = q & 1;
        fw[j][ks][hf] = lds_tr(smem + (j == 6 ? boffM : boffP) + bbuf * K3_BB + (j < 3 ? j * 32 : (j < 6 ? K3_UOFF + (j - 3) * 32 : 0)) + ks * 7680 + hf * 896);
      } else fx[q - 14][ks] = lds_b128(smem + (aoff0 ^ (ks << 6)) + abuf * K3_AH + (q - 14) * 2048);
    };
    f32x4 acc[4][7];
#pragma unroll
    for (int m = 0; m < 4; ++m)
#pragma unroll
      for (int j = 0; j < 7; ++j) zero_pin(acc[m][j]);
    auto mma = [&]() {
      __builtin_amdgcn_s_setprio(1);
#pragma unroll
      for (int ks = 0; ks < 2; ++ks)
#pragma unroll
        for (int j = 0; j < 7; ++j)
#pragma unroll
          for (int m = 0; m < 4; ++m) mfma_ip(acc[m][j], cat8(fw[j][ks][0], fw[j][ks][1]), fx[m][ks]);
      __builtin_amdgcn_s_setprio(0);
    };
    constexpr int NT = D_ / 64;
#pragma unroll
    for (int j = 0; j < 4; ++j) pin(voffB[j]);
    if (w == 0) wait_panel(cnt + (16 + tm3) * 64, 32, cnt + 32 * 64, lane);
    bar();
    acquire_agent();
    TLS(3, 0);
#pragma unroll
    for (int j = 0; j < 4; ++j) dmaA(0, grp, 0, j);
    if (grp == 0) {
      wait_vm<0>(); bar();
    } else {
      wait_vm<4>(); bar();
      float st[16];
      const float* sp = ssq + r3 + (tid & 255);
#pragma unroll
      for (int i = 0; i < 16; ++i) asm volatile("global_load_dword %0, %1, off sc1" : "=v"(st[i]) : "v"(sp + i * S_));
#pragma unroll
      for (int j = 0; j < 4; ++j) { dmaA(1, 0, 1, j); dmaB(1, 1, j); }
      asm volatile("s_waitcnt vmcnt(7)" : "+v"(st[0]), "+v"(st[1]), "+v"(st[2]), "+v"(st[3]), "+v"(st[4]), "+v"(st[5]), "+v"(st[6]), "+v"(st[7]),
                   "+v"(st[8]), "+v"(st[9]), "+v"(st[10]), "+v"(st[11]), "+v"(st[12]), "+v"(st[13]), "+v"(st[14]), "+v"(st[15]) :: "memory");
      float q = 0.f;
#pragma unroll
      for (int i = 0; i < 16; ++i) q += st[i];
      *(LAS float*)(smem + K3_TAB + (tid & 255) * 4) = ln_rstd(q * (1.f / D_));
      wait_lgkm0(); bar();
    }
    mfma_fence(); SB();
    const int ahalf = grp ^ 1;
    sfor<0, NT>([&](auto t_c) {
      constexpr int t = decltype(t_c)::v, ab = t & 1, bb = t % 3;
      const int abuf = ab ^ ahalf;
      sfor<0, 36>([&](auto r) {
        constexpr int rr = decltype(r)::v;
        rd(ab, bb, r);
        if constexpr (t < NT - 2 && (rr & 3) == 3 && rr < 32) {
          constexpr int d = rr >> 2;
          if constexpr (d & 1) dmaB(t + 2, (bb + 2) % 3, d >> 1); else dmaA(t + 1 + grp, ahalf, abuf, d >> 1);
        }
        if constexpr (t == NT - 2 && (rr & 7) == 3 && rr < 32) { if (grp == 0) dmaA(t + 1, 1, ab ^ 1, rr >> 3); }
        if constexpr (t < NT - 2) SB();
      });
      if constexpr (t == NT - 2)
        dma16(lds0 + K3_DUMMY, lane < 28 ? (unsigned)lane * 128u : OOB, make_srd(wd, D_ * F_ * 2), (unsigned)bid * 28672u + w * 3584u);
      wait_lgkm0();
      if constexpr (t < NT - 2) wait_vm<7>();
      else if constexpr (t == NT - 2) { if (grp == 0) wait_vm<5>(); else wait_vm<1>(); }
      else wait_vm<1>();
      bar(); SB();
      mma();
      SB();
      if constexpr (t < NT - 1) bar(); else { if (grp == 0) bar(); }
      SB();
    });
    mfma_fence(); SB();
    TLS(3, 2);
    const float C0 = -2.0f * 1.4426950408889634f * 0.7978845608028654f, C1 = C0 * 0.044715f;
    auto geglu2 = [&](f32x2 gt, f32x2 up, float c0a, float c1a, float ia2) {
      const f32x2 z = gt * (gt * gt * c1a + c0a);
      f32x2 e; e.x = __builtin_amdgcn_exp2f(z.x); e.y = __builtin_amdgcn_exp2f(z.y);
      const f32x2 d = e * ia2 + ia2;
      f32x2 r; r.x = __builtin_amdgcn_rcpf(d.x); r.y = __builtin_amdgcn_rcpf(d.y);
      const f32x2 o2 = gt * up * r;
      return pk_bf16(o2.x, o2.y);
    };
    const int rowb = 128 * grp + 64 * wm;
    LAS char* ast = smem + w * 8192 + c * 128 + 8 * (g & 1);
    const int csw = c & 7;
#pragma unroll
    for (int mp = 0; mp < 2; ++mp) {
      const int mrow = 2 * mp + (g >> 1);
      const float a = *(const LAS float*)(smem + K3_TAB + (rowb + 16 * mrow + c) * 4);
      const float c0a = C0 * a, c1a = C1 * a * a * a, ia2 = __builtin_amdgcn_rcpf(a * a);
      float gt[4], up[4];
#pragma unroll
      for (int r = 0; r < 4; ++r) {
        auto s = __builtin_amdgcn_permlane32_swap(__float_as_uint(acc[2 * mp][6][r]), __float_as_uint(acc[2 * mp + 1][6][r]), false, false);
        gt[r] = __uint_as_float(s[0]); up[r] = __uint_as_float(s[1]);
      }
      *(LAS u32x2*)(ast + mrow * 2048 + ((6 ^ csw) << 4)) =
          (u32x2){geglu2((f32x2){gt[0], gt[1]}, (f32x2){up[0], up[1]}, c0a, c1a, ia2), geglu2((f32x2){gt[2], gt[3]}, (f32x2){up[2], up[3]}, c0a, c1a, ia2)};
      *(LAS u32x2*)(ast + mrow * 2048 + ((7 ^ csw) << 4)) = (u32x2){0u, 0u};
    }
    char* actw = (char*)act + (size_t)(2 * tn3 + wn) * KT_ + (r3 + rowb + lr) * 128 + ((lc ^ lr) << 4);
#pragma unroll
    for (int m = 0; m < 4; ++m) {
      const float a = *(const LAS float*)(smem + K3_TAB + (rowb + 16 * m + c) * 4);
      const float c0a = C0 * a, c1a = C1 * a * a * a, ia2 = __builtin_amdgcn_rcpf(a * a);
#pragma unroll
      for (int b = 0; b < 3; ++b) {
        const f32x4 gt = acc[m][b], up = acc[m][3 + b];
        *(LAS u32x2*)(ast + m * 2048 + (((2 * b + (g >> 1)) ^ csw) << 4)) =
            (u32x2){geglu2((f32x2){gt[0], gt[1]}, (f32x2){up[0], up[1]}, c0a, c1a, ia2), geglu2((f32x2){gt[2], gt[3]}, (f32x2){up[2], up[3]}, c0a, c1a, ia2)};
      }
#pragma unroll
      for (int i = 0; i < 2; ++i) st_wt(actw + (2 * m + i) * 1024, *(const LAS u32x4*)(smem + w * 8192 + (2 * m + i) * 1024 + lane * 16));
    }
    TLS(3, 3);
  }
  {
    constexpr int NX = 32 - P4_D, NS4 = 32 + P4_D;
    const int kh = tn & 1;
    const i32x4 srdW = make_srd(wd, F_ * D_ * 2);
    const int fW = ((lane >> 4) & 1) | ((w4 & 1) << 1);
    const unsigned voffW0 = (unsigned)((8 * w4 + lr) * (D_ * 2) + (((((lc >> 1) ^ fW) << 1) | (lc & 1)) << 4));
    const bool w7 = w4 != 3;
    const unsigned stx = kh ? (unsigned)NS4 : 0u;
    const int cls4 = cls;
    auto stg = [&](int s) { return s < NX ? kord4(s, NX, cls4) + stx : kord4(s - NX, 2 * P4_D, cls4) + NX; };
    auto issueW = [&](auto s_c) {
      constexpr int s = decltype(s_c)::v;
      const unsigned T = stg(s), lb = lds0 + P4_W + (s % P4_NSW) * 16384 + w4 * 1024, so = T * (56 * D_ * 2);
      dma16(lb, voffW0, srdW, so + tn * 128);
      if constexpr (s < NX) dma16(lb + 8192, voffW0, srdW, so + (tn ^ 1) * 128);
      if (w7) {
        dma16(lb + 4096, voffW0, srdW, so + 32 * D_ * 2 + tn * 128);
        if constexpr (s < NX) dma16(lb + 12288, voffW0, srdW, so + 32 * D_ * 2 + (tn ^ 1) * 128);
      }
    };
    wait_lgkm0(); wait_vm<0>();
    if (w < 4) {
      sfor<0, P4_NSW>([&](auto s) { issueW(s); });
      for (int i = w; i < 2 * P4_NSW; i += 4) *(LAS u32x4*)(smem + P4_W + i * 8192 + 7168 + lane * 16) = (u32x4){0u, 0u, 0u, 0u};
      wait_lgkm0();
    }
    bar();
    if (w == 4) { signal_panel(cnt + (24 + tm3) * 64, lane); wait_panel(cnt + (24 + (tm >> 1)) * 64, 32, cnt + 32 * 64, lane); }
    bar();
    TLS(4, 0);
    if (w >= 4) {
      acquire_agent();
      const i32x4 srdA = make_srd(act, 64 * KT_);
      const unsigned voffA = (unsigned)((8 * w4 + lr) * 128 + ((lc ^ ((4 * w4 + (lane >> 4)) & 7)) << 4)), sA = (unsigned)r0 * 128;
      auto issueA = [&](auto s_c) {
        constexpr int s = decltype(s_c)::v;
        const unsigned T = stg(s);
#pragma unroll
        for (int j = 0; j < 4; ++j) dma16_dev(lds0 + (s % G_NSA) * 16384 + w4 * 1024 + j * 4096, voffA, srdA, sA + j * 4096 + T * KT_);
      };
      issueA(IC<0>{}); issueA(IC<1>{});
      wait_vm<4>(); bar();
      issueA(IC<2>{}); issueA(IC<3>{});
      sfor<0, NS4 - 1>([&](auto s_c) {
        constexpr int s = decltype(s_c)::v;
        constexpr auto nw = [](int st) { return (st < 0 || st + P4_NSW >= NS4) ? 0 : (st + P4_NSW < NX ? 2 : 1); };
        constexpr int n = nw(s - 2) + nw(s - 1) + (s + 2 < NS4 ? 4 : 0) + (s + 3 < NS4 ? 4 : 0);
        wait_vm<n>(); bar();
        if constexpr (s + P4_NSW < NS4) issueW(IC<s + P4_NSW>{});
        if constexpr (s + 4 < NS4) issueA(IC<s + 4>{});
      });
    } else {
      __builtin_amdgcn_s_setprio(1);
      const int wm = w >> 1, wo_ = w & 1;
      const int sw = (lane >> 1) & 7;
      const int aoff0 = (64 * wm + c) * 128 + ((g ^ sw) << 4);
      const int q4 = c >> 2, p4 = lane & 3, fR = ((q4 >> 1) & 1) | ((g & 1) << 1);
      const int woff0 = P4_W + wo_ * 8192 + (8 * g + q4) * 128 + (fR << 5) + 8 * p4;
      auto rd = [&](P4Frag& f, auto s_c, auto r_c) {
        constexpr int s = decltype(s_c)::v, r = decltype(r_c)::v, ks = r / 12, q = r % 12;
        if constexpr (q < 4) f.x[q][ks] = lds_b128(smem + (s % G_NSA) * 16384 + (aoff0 ^ (ks << 6)) + q * 2048);
        else f.w[(q - 4) >> 1][ks][q & 1] = lds_tr(smem + (s % P4_NSW) * 16384 + (woff0 ^ (((q - 4) >> 1) << 5)) + ks * 4096 + (q & 1) * 512);
      };
      f32x4 acc[4][4];
#pragma unroll
      for (int m = 0; m < 4; ++m)
#pragma unroll
        for (int j = 0; j < 4; ++j) zero_pin(acc[m][j]);
      P4Frag fr[2];
      auto step = [&](auto s_c, auto pre_c) {
        constexpr int s = decltype(s_c)::v;
        sfor<0, 32>([&](auto i_c) {
          constexpr int i = decltype(i_c)::v, ks = i >> 4, j = (i >> 2) & 3, m = i & 3;
          if constexpr (decltype(pre_c)::v && i < 24) rd(fr[(s + 1) & 1], IC<s + 1>{}, i_c);
          mfma_ip(acc[m][j], cat8(fr[s & 1].w[j][ks][0], fr[s & 1].w[j][ks][1]), fr[s & 1].x[m][ks]);
          SB();
        });
      };
      wait_vm<0>();
      bar();
      TLS(4, 1);
      sfor<0, 24>([&](auto r) { rd(fr[0], IC<0>{}, r); });
      mfma_fence(); SB();
      sfor<0, NX>([&](auto s) { wait_lgkm0(); bar(); SB(); step(s, IC<1>{}); });
      f32x4* const part = (f32x4*)(ws + WS_PART);
      mfma_fence(); SB();
      if (wo_) {
#pragma unroll
        for (int i = 0; i < 16; ++i) st_wt(part + ((bid * 2 + wm) * 16 + i) * 64 + lane, acc[i >> 2][i & 3]);
        sfor<NX, NS4 - 1>([&](auto s_c) {
          if constexpr (decltype(s_c)::v == NX + P4_PUB) {
            wait_vm<0>();
            if (lane == 0) __hip_atomic_store((gu32*)(cnt + CNT_PF + (bid * 2 + wm) * 32), 1u, __ATOMIC_RELAXED, __HIP_MEMORY_SCOPE_AGENT);
          }
          bar();
        });
      } else {
        sfor<NX, NS4 - 1>([&](auto s) { wait_lgkm0(); bar(); SB(); step(s, IC<1>{}); });
        const int pb = bid ^ 1;
        unsigned* const pf = cnt + CNT_PF + (pb * 2 + wm) * 32;
        unsigned fl;
        ld_async_dev(fl, pf);
        const int ecol = tn * 64 + ((g & 1) << 4) + ((g >> 1) << 3);
        u32x4 res[4][2];
#pragma unroll
        for (int m = 0; m < 4; ++m)
#pragma unroll
          for (int jp = 0; jp < 2; ++jp) ld_async(res[m][jp], h + (size_t)(r0 + 64 * wm + 16 * m + c) * D_ + ecol + 32 * jp);
        SB();
        step(IC<NS4 - 1>{}, IC<0>{});
        mfma_fence();
        asm volatile("s_waitcnt vmcnt(8)" : "+v"(fl) :: "memory");
        TLS(4, 2);
        if (__builtin_amdgcn_readfirstlane(fl) == 0u) {
          for (unsigned spins = 0;; ++spins) {
            if (__builtin_amdgcn_readfirstlane(__hip_atomic_load((gu32*)pf, __ATOMIC_RELAXED, __HIP_MEMORY_SCOPE_AGENT)) != 0u) break;
            if (spins > (1u << 15)) { if (lane == 0) __hip_atomic_store((gu32*)(cnt + 32 * 64), 1u, __ATOMIC_RELAXED, __HIP_MEMORY_SCOPE_AGENT); break; }
            __builtin_amdgcn_s_sleep(1);
          }
        }
        f32x4 pp[16];
#pragma unroll
        for (int i = 0; i < 16; ++i) ld_async_dev(pp[i], part + ((pb * 2 + wm) * 16 + i) * 64 + lane);
        asm volatile("s_waitcnt vmcnt(0)" : "+v"(pp[0]), "+v"(pp[1]), "+v"(pp[2]), "+v"(pp[3]), "+v"(pp[4]), "+v"(pp[5]), "+v"(pp[6]), "+v"(pp[7]),
                     "+v"(pp[8]), "+v"(pp[9]), "+v"(pp[10]), "+v"(pp[11]), "+v"(pp[12]), "+v"(pp[13]), "+v"(pp[14]), "+v"(pp[15]),
                     "+v"(res[0][0]), "+v"(res[0][1]), "+v"(res[1][0]), "+v"(res[1][1]), "+v"(res[2][0]), "+v"(res[2][1]), "+v"(res[3][0]), "+v"(res[3][1]) :: "memory");
#pragma unroll
        for (int m = 0; m < 4; ++m)
#pragma unroll
          for (int jp = 0; jp < 2; ++jp) {
            const u32x2 a0 = pk4(acc[m][2 * jp] + pp[4 * m + 2 * jp]), a1 = pk4(acc[m][2 * jp + 1] + pp[4 * m + 2 * jp + 1]);
            auto e0 = __builtin_amdgcn_permlane16_swap(a0[0], a1[0], false, false);
            auto e1 = __builtin_amdgcn_permlane16_swap(a0[1], a1[1], false, false);
            const unsigned e[4] = {e0[0], e1[0], e0[1], e1[1]};
            u32x4 ov;
#pragma unroll
            for (int i = 0; i < 4; ++i) ov[i] = pk_bf16(bf_lo(e[i]) + bf_lo(res[m][jp][i]), bf_hi(e[i]) + bf_hi(res[m][jp][i]));
            *(u32x4*)(out + (size_t)(r0 + 64 * wm + 16 * m + c) * D_ + ecol + 32 * jp) = ov;
          }
        TLS(4, 3);
      }
    }
  }
}

extern "C" void kernel_launch(void* const* d_in, const int* in_sizes, int n_in, void* d_out, int out_size, void* d_ws, size_t ws_size,
                              hipStream_t stream) {
  const u16* x = (const u16*)d_in[0];
  const u16* wqkv = (const u16*)d_in[1];
  const u16* wo = (const u16*)d_in[2];
  const u16* wg = (const u16*)d_in[4];
  const u16* wu = (const u16*)d_in[5];
  const u16* wd = (const u16*)d_in[6];
  char* ws = (char*)d_ws + ((ws_size / 2) & ~(size_t)0xfffff);
  k0_ln<<<256, 512, 0, stream>>>(x, (u16*)(ws + WS_XN), (float*)(ws + WS_XMEAN), wo, (u16*)(ws + WS_WBAR), wqkv, (unsigned*)(ws + WS_CNT));
  km_block<<<256, 512, 0, stream>>>(x, wqkv, wo, wg, wu, wd, (u16*)d_out, ws);
}
```

```cpp
#include <hip/hip_runtime.h>
#include <hip/hip_bf16.h>

typedef unsigned short u16;
typedef unsigned long long u64;
typedef __attribute__((ext_vector_type(8))) __bf16 bf16x8;
typedef __attribute__((ext_vector_type(2))) __bf16 bf16x2;
typedef __attribute__((ext_vector_type(4))) short s16x4;
typedef __attribute__((ext_vector_type(4))) float f32x4;
typedef __attribute__((ext_vector_type(2))) float f32x2;
typedef __attribute__((ext_vector_type(4))) int i32x4;
typedef __attribute__((ext_vector_type(4))) unsigned u32x4;
typedef __attribute__((ext_vector_type(2))) unsigned u32x2;
#define LAS __attribute__((address_space(3)))
#define DEVI __device__ __forceinline__

constexpr int S_ = 2048, D_ = 1024, F_ = 3584;
constexpr size_t WS_HC = 0, WS_O = 4u << 20, WS_H = 8u << 20, WS_SSQ = 12u << 20, WS_STAT = 13u << 20, WS_WBAR = 14u << 20, WS_FLG = 15u << 20,
                 WS_ACT = 16u << 20, WS_PART = 40u << 20, WS_WDC = 48u << 20;
#define FSTR 32
constexpr int FS = FSTR;
constexpr int FL_1 = 512, FL_2 = FL_1 + 256 * FS, FL_3 = FL_2 + 256 * FS;
constexpr unsigned CH_ = S_ * 16, KT_ = 8 * CH_;
constexpr unsigned OOB = 0x80000000u;

DEVI void wait_lgkm0() { asm volatile("s_waitcnt lgkmcnt(0)" ::: "memory"); }
template <int N> struct IC { static constexpr int v = N; };
template <int I, int N, class F> DEVI void sfor(F&& f) { if constexpr (I < N) { f(IC<I>{}); sfor<I + 1, N>(f); } }
#define TLR(n) do {} while (0)
#define TLW(k) do {} while (0)
#define TLS(k, i) do {} while (0)

DEVI i32x4 make_srd(const void* p, unsigned bytes) {
  unsigned long long a = (unsigned long long)p;
  i32x4 r;
  r.x = (int)(unsigned)a; r.y = (int)((unsigned)(a >> 32) & 0xffffu); r.z = (int)bytes; r.w = 0x00020000;
  return r;
}
DEVI i32x4 code_srd() {
  u64 pc; unsigned off, bytes;
  asm volatile("s_getpc_b64 %0\n2:\n\ts_mov_b32 %1, 2b-.Lkm_first\n\ts_mov_b32 %2, .Lkm_last-.Lkm_first" : "=s"(pc), "=s"(off), "=s"(bytes));
  return make_srd((const void*)(pc - (u64)(long long)(int)off), bytes);
}
DEVI void dma16(unsigned lds_addr, unsigned voff, i32x4 srd, unsigned soff) {
  asm volatile("s_mov_b32 m0, %0\n\ts_nop 0\n\tbuffer_load_dwordx4 %1, %2, %3 offen lds"
               :: "s"(lds_addr), "v"(voff), "s"(srd), "s"(soff));
}
DEVI void dma16_dev_if0(int cond, unsigned lds_addr, unsigned voff, i32x4 srd, unsigned soff) {
  asm volatile("s_cmp_lg_u32 %4, 0\n\ts_cbranch_scc1 1f\n\ts_mov_b32 m0, %0\n\ts_nop 0\n\tbuffer_load_dwordx4 %1, %2, %3 offen sc1 lds\n1:"
               :: "s"(lds_addr), "v"(voff), "s"(srd), "s"(soff), "s"(__builtin_amdgcn_readfirstlane(cond)) : "scc");
}
DEVI void dma16_lanes(u64 mask, unsigned lds_addr, unsigned voff, i32x4 srd, unsigned soff) {
  asm volatile("s_mov_b32 m0, %0\n\ts_mov_b64 exec, %4\n\tbuffer_load_dwordx4 %1, %2, %3 offen lds\n\ts_mov_b64 exec, -1"
               :: "s"(lds_addr), "v"(voff), "s"(srd), "s"(soff), "s"(mask));
}
DEVI void dma16_dev(unsigned lds_addr, unsigned voff, i32x4 srd, unsigned soff) {
  asm volatile("s_mov_b32 m0, %0\n\ts_nop 0\n\tbuffer_load_dwordx4 %1, %2, %3 offen sc1 lds"
               :: "s"(lds_addr), "v"(voff), "s"(srd), "s"(soff));
}
template <int N> DEVI void wait_vm() { asm volatile("s_waitcnt vmcnt(%0)" :: "n"(N) : "memory"); }
DEVI void bar() { asm volatile("" ::: "memory"); __builtin_amdgcn_s_barrier(); asm volatile("" ::: "memory"); }
#define SB() __builtin_amdgcn_sched_barrier(0)
#define NOUNROLL _Pragma("clang loop unroll(disable)")
#define PAGE(a, b) asm volatile("s_branch 1f\n\t.p2align 12\n.Lkm_p" #a ":\n\ts_branch .Lkm_" #b "\n1:")
#define PAD(a, b) asm volatile("s_branch 1f\n.Lkm_p" #a ":\n\ts_branch .Lkm_" #b "\n1:")
#define VISIT4(w_, a, b, c, d, r) asm volatile("s_cmp_eq_u32 %0, 0\n\ts_cbranch_scc1 .Lkm_p" #a "\n\ts_cmp_eq_u32 %0, 1\n\ts_cbranch_scc1 .Lkm_p" #b \
  "\n\ts_cmp_eq_u32 %0, 2\n\ts_cbranch_scc1 .Lkm_p" #c "\n\ts_branch .Lkm_p" #d "\n.Lkm_" #r ":" :: "s"(w_) : "scc")

DEVI void st_wt(void* p, u32x4 v) { asm volatile("global_store_dwordx4 %0, %1, off sc1\n\ts_nop 1" :: "v"(p), "v"(v) : "memory"); }
DEVI void st_wt(void* p, f32x4 v) { asm volatile("global_store_dwordx4 %0, %1, off sc1\n\ts_nop 1" :: "v"(p), "v"(v) : "memory"); }
template <int OFF> DEVI void st_wt(void* p, u32x4 v) { asm volatile("global_store_dwordx4 %0, %1, off offset:%2 sc1\n\ts_nop 1" :: "v"(p), "v"(v), "n"(OFF) : "memory"); }
DEVI void st_plain(void* p, u32x4 v) { asm volatile("global_store_dwordx4 %0, %1, off\n\ts_nop 1" :: "v"(p), "v"(v) : "memory"); }
DEVI void st_wt_lanes(u64 mask, void* p, u32x4 v) {
  asm volatile("s_mov_b64 exec, %2\n\tglobal_store_dwordx4 %0, %1, off sc1\n\ts_mov_b64 exec, -1\n\ts_nop 0" :: "v"(p), "v"(v), "s"(mask) : "memory");
}
DEVI void ld_async(u32x4& d, const void* p) { asm volatile("global_load_dwordx4 %0, %1, off" : "=v"(d) : "v"(p)); }
DEVI void ld_async_dev(f32x4& d, const void* p) { asm volatile("global_load_dwordx4 %0, %1, off sc1" : "=v"(d) : "v"(p)); }
DEVI void ld_async_dev(u32x4& d, const void* p) { asm volatile("global_load_dwordx4 %0, %1, off sc1" : "=v"(d) : "v"(p)); }
DEVI void ld_async_dev(u32x2& d, const void* p) { asm volatile("global_load_dwordx2 %0, %1, off sc1" : "=v"(d) : "v"(p)); }
template <int OFF> DEVI void ldb_async(u32x4& d, unsigned voff, i32x4 srd, unsigned soff) {
  asm volatile("buffer_load_dwordx4 %0, %1, %2, %3 offen offset:%4" : "=v"(d) : "v"(voff), "s"(srd), "s"(soff), "n"(OFF));
}
template <int OFF> DEVI void ldb_dev(bf16x8& d, unsigned voff, i32x4 srd, unsigned soff) {
  asm volatile("buffer_load_dwordx4 %0, %1, %2, %3 offen offset:%4 sc1" : "=v"(d) : "v"(voff), "s"(srd), "s"(soff), "n"(OFF));
}
DEVI void st_wt(void* p, u32x2 v) { asm volatile("global_store_dwordx2 %0, %1, off sc1" :: "v"(p), "v"(v) : "memory"); }
DEVI void st_wt(void* p, float v) { asm volatile("global_store_dword %0, %1, off sc1" :: "v"(p), "v"(v) : "memory"); }
DEVI bf16x8 lds_b128(const LAS char* p) { return *(const LAS bf16x8*)p; }
DEVI s16x4 lds_tr(const LAS char* p) { return __builtin_amdgcn_ds_read_tr16_b64_v4i16((LAS s16x4*)p); }
DEVI bf16x8 cat8(s16x4 a, s16x4 b) { return __builtin_bit_cast(bf16x8, __builtin_shufflevector(a, b, 0, 1, 2, 3, 4, 5, 6, 7)); }
DEVI bf16x8 cat8u(u32x2 a, u32x2 b) { return __builtin_bit_cast(bf16x8, (u32x4){a[0], a[1], b[0], b[1]}); }
DEVI f32x4 mfma(bf16x8 a, bf16x8 b, f32x4 c) { return __builtin_amdgcn_mfma_f32_16x16x32_bf16(a, b, c, 0, 0, 0); }
DEVI f32x4 mfma16(u32x2 a, u32x2 b, f32x4 c) { return __builtin_amdgcn_mfma_f32_16x16x16bf16_1k(__builtin_bit_cast(s16x4, a), __builtin_bit_cast(s16x4, b), c, 0, 0, 0); }
DEVI void mfma_ip(f32x4& c, bf16x8 a, bf16x8 b) { asm volatile("v_mfma_f32_16x16x32_bf16 %0, %1, %2, %0" : "+v"(c) : "v"(a), "v"(b)); }
template <class T> DEVI void pin(T& v) { asm volatile("" : "+v"(v)); }
DEVI void zero_pin(f32x4& v) { v = (f32x4){0, 0, 0, 0}; pin(v); }
DEVI void mfma_fence() { asm volatile("s_nop 7\n\ts_nop 7" ::: "memory"); }
DEVI unsigned pk_bf16(float lo, float hi) { bf16x2 v = {(__bf16)lo, (__bf16)hi}; return __builtin_bit_cast(unsigned, v); }
DEVI u32x2 pk4(f32x4 v) { return (u32x2){pk_bf16(v[0], v[1]), pk_bf16(v[2], v[3])}; }
DEVI float bf_lo(unsigned u) { return __uint_as_float(u << 16); }
DEVI float bf_hi(unsigned u) { return __uint_as_float(u & 0xffff0000u); }
DEVI unsigned umax(unsigned a, unsigned b) { return a > b ? a : b; }
DEVI int fresh_lane() { int l; asm volatile("v_mbcnt_lo_u32_b32 %0, -1, 0\n\tv_mbcnt_hi_u32_b32 %0, -1, %0" : "=v"(l)); return l; }
#define LANE_IDS() const int lane = fresh_lane(), lr = lane >> 3, lc = lane & 7, g = lane >> 4, c = lane & 15; (void)lr; (void)lc; (void)g; (void)c
DEVI float rbf(float x) { return (float)(__bf16)x; }
DEVI float red16_add(float x) { auto s = __builtin_amdgcn_permlane16_swap(__float_as_uint(x), __float_as_uint(x), false, false); return __uint_as_float(s[0]) + __uint_as_float(s[1]); }
DEVI float red32_add(float x) { auto s = __builtin_amdgcn_permlane32_swap(__float_as_uint(x), __float_as_uint(x), false, false); return __uint_as_float(s[0]) + __uint_as_float(s[1]); }
DEVI float vmax(float a, float b) { float r; asm("v_max_f32 %0, %1, %2" : "=v"(r) : "v"(a), "v"(b)); return r; }
DEVI float vmax3(float a, float b, float c) { float r; asm("v_max3_f32 %0, %1, %2, %3" : "=v"(r) : "v"(a), "v"(b), "v"(c)); return r; }
DEVI float red16_max(float x) { auto s = __builtin_amdgcn_permlane16_swap(__float_as_uint(x), __float_as_uint(x), false, false); return fmaxf(__uint_as_float(s[0]), __uint_as_float(s[1])); }
DEVI float red32_max(float x) { auto s = __builtin_amdgcn_permlane32_swap(__float_as_uint(x), __float_as_uint(x), false, false); return fmaxf(__uint_as_float(s[0]), __uint_as_float(s[1])); }
DEVI float ln_rstd(float var) { return __builtin_amdgcn_rcpf(rbf(__builtin_amdgcn_sqrtf(rbf(rbf(var) + 1e-5f)))); }

DEVI float wave_sum(float x) {
  x += __builtin_bit_cast(float, __builtin_amdgcn_update_dpp(0, __builtin_bit_cast(int, x), 0xB1, 0xf, 0xf, true));
  x += __builtin_bit_cast(float, __builtin_amdgcn_update_dpp(0, __builtin_bit_cast(int, x), 0x4E, 0xf, 0xf, true));
  x += __builtin_bit_cast(float, __builtin_amdgcn_update_dpp(0, __builtin_bit_cast(int, x), 0x141, 0xf, 0xf, true));
  x += __builtin_bit_cast(float, __builtin_amdgcn_update_dpp(0, __builtin_bit_cast(int, x), 0x140, 0xf, 0xf, true));
  return red32_add(red16_add(x));
}

typedef __attribute__((address_space(1))) u64 gu64;
constexpr unsigned SPIN_MAX = 1u << 15;
DEVI void signal_flag(u64* f, u64 tok) {
  if (fresh_lane() == 0) __hip_atomic_store((gu64*)f, tok, __ATOMIC_RELAXED, __HIP_MEMORY_SCOPE_AGENT);
}
DEVI void wait_flags(u64* f, int n, u64 tok) {
  const int lane = fresh_lane();
  NOUNROLL for (unsigned spins = 0; spins < SPIN_MAX; ++spins) {
    if (__builtin_amdgcn_ballot_w64(__hip_atomic_load((gu64*)f + (lane & (n - 1)) * FS, __ATOMIC_RELAXED, __HIP_MEMORY_SCOPE_AGENT) != tok) == 0) break;
    __builtin_amdgcn_s_sleep(1);
  }
}
DEVI void settle_flags(u64& v, const u64* p, u64 need, u64 tok) {
  unsigned cnt;
  asm volatile("s_movk_i32 %1, 0x7fff\n1:\n\tv_cmp_ne_u64 vcc, %4, %0\n\ts_and_b64 vcc, vcc, %3\n\ts_cbranch_scc0 9f\n\ts_sub_u32 %1, %1, 1\n\ts_cbranch_scc1 9f\n\t"
               "s_sleep 1\n\tglobal_load_dwordx2 %0, %2, off sc1\n\ts_waitcnt vmcnt(0)\n\ts_branch 1b\n9:"
               : "+v"(v), "=&s"(cnt) : "v"(p), "s"(need), "s"(tok) : "vcc", "scc", "memory");
}
#define PSL 3
#define PW3 1
struct Poll { u64 a, b, c; };
DEVI void poll_flags(Poll& v, const u64* p, u64 need, u64 tok) {
  unsigned cnt;
#define POLL_STEP(n, o) "global_load_dwordx2 %" #n ", %4, off sc1\n\ts_waitcnt vmcnt(2)\n\tv_cmp_ne_u64 vcc, %6, %" #o "\n\ts_cbranch_vccz 9f\n\t"
  asm volatile("s_mov_b64 exec, %5\n\ts_movk_i32 %3, 0x7fff\n\tglobal_load_dwordx2 %0, %4, off sc1\n\ts_sleep %7\n\tglobal_load_dwordx2 %1, %4, off sc1\n\ts_sleep %7\n"
               "1:\n\t" POLL_STEP(2, 0) POLL_STEP(0, 1) POLL_STEP(1, 2) "s_sub_u32 %3, %3, 1\n\ts_cbranch_scc0 1b\n9:\n\ts_mov_b64 exec, -1"
               : "+v"(v.a), "+v"(v.b), "+v"(v.c), "=&s"(cnt) : "v"(p), "s"(need), "s"(tok), "n"(PSL) : "vcc", "scc", "memory");
}
DEVI void poll_regs(Poll& v) { asm volatile("" : "=v"(v.a), "=v"(v.b), "=v"(v.c)); }
DEVI void poll_done(Poll& v) { asm volatile("" :: "v"(v.a), "v"(v.b), "v"(v.c)); }
DEVI void settle(u32x4& v, const void* p, unsigned tag) {
  NOUNROLL for (unsigned spins = 0; spins < SPIN_MAX && __builtin_amdgcn_ballot_w64(v[1] != tag || v[3] != tag) != 0; ++spins) {
    __builtin_amdgcn_s_sleep(1);
    ld_async_dev(v, p);
    asm volatile("s_waitcnt vmcnt(0)" : "+v"(v) :: "memory");
  }
}
DEVI void acquire_agent() { asm volatile("" ::: "memory"); }

DEVI unsigned p2_tile(int s, int tn) { return (unsigned)(((((tn >> 1) + 1 + s) & 7) << 1) | (((s >> 3) ^ tn) & 1)); }
constexpr int K1_AS = 18432, K1_WS = 24576;
constexpr int K1_W0 = 3 * K1_AS;
constexpr int K1_DUMMY = K1_W0 + 4 * K1_WS;
constexpr int K1_STT = K1_DUMMY + 1024, K1_CST = K1_STT + 2048;
constexpr int K1_EX = 98304;
constexpr int K1_SC = K1_AS;
constexpr int P2_W = 113664, P2_W2 = 65536;
constexpr int P2_WB = 146432;
constexpr int L_SYNC = 162816;
constexpr int G_NSA = 4;
constexpr int K3_AH = 16384;
constexpr int K3_UOFF = 15360 + 16;
constexpr int K3_BB = K3_UOFF + 15360;
constexpr int K3_B0 = 4 * K3_AH;
constexpr int K3_TAB = K3_B0 + 3 * K3_BB;
constexpr int K3_DUMMY = K3_TAB + 1024, K3_FL = K3_DUMMY + 1024, T_FL = 9;
constexpr int P4_W = 65536, P4_INB = P4_W + 5 * 16384;
#define YD 5
#define P2_LEAD 3
#define P2_NF 8
#define P2_TB 12
#define NWX 4
#define TCH 5
#define HW 4
#define TB 8
#define P2_NB 2
#define P4_IN 3
#define TS_ 2
constexpr int T_S = TS_, T_WO = 10;
constexpr int T_G2 = 1, T_CT = 12, T_WD = 14;
constexpr int KM_LDS = 163840;

constexpr float SL2E = 0.17677669529663687f * 1.4426950408889634f;
struct P2Frag { bf16x8 wb; s16x4 w[4][2]; };

DEVI void p2_compute(LAS char* smem, int w, int lane, int r0, int c0, int tn, const u16* __restrict__ R, const u16* A, u16* __restrict__ O,
                     u16* __restrict__ OC, float* __restrict__ ssq, const float (&xm)[2], const u64* flags, u64 tok) {
  constexpr int NT = D_ / 64, LEAD = P2_LEAD;
  const int g = lane >> 4, c = lane & 15;
  const int ecol = c0 + ((g & 1) << 4) + ((g >> 1) << 3);
  u32x4 res[2][2];
#pragma unroll
  for (int m = 0; m < 2; ++m)
#pragma unroll
    for (int jp = 0; jp < 2; ++jp) ld_async(res[m][jp], R + (size_t)(r0 + 32 * w + 16 * m + c) * D_ + ecol + 32 * jp);
  const int q4 = c >> 2, p4 = lane & 3, fR = ((q4 >> 1) & 1) | ((g & 1) << 1);
  const int woff0 = (8 * g + q4) * 128 + (fR << 5) + 8 * p4;
  const int wboff = P2_WB + (c & 1) * 2048 + g * 16;
  int wb[3][4];
#pragma unroll
  for (int i = 0; i < 12; ++i) { wb[i >> 2][i & 3] = (woff0 ^ ((i & 3) << 5)) + (i < 4 ? P2_W2 : (i < 8 ? P2_W : 0)); pin(wb[i >> 2][i & 3]); }
  auto rd = [&](P2Frag& f, auto t_c, int ks, auto r_c) {
    constexpr int t = decltype(t_c)::v, r = decltype(r_c)::v;
    if constexpr (r == 8) f.wb = lds_b128(smem + wboff + t * 128 + ks * 64);
    else f.w[r >> 1][r & 1] = lds_tr(smem + wb[t < 8 ? t >> 2 : 2][r >> 1] + (t < 8 ? t & 3 : t - 8) * 8192 + ks * 4096 + (r & 1) * 512);
  };
  const i32x4 srdA = make_srd(A, S_ * D_ * 2);
  unsigned voffA[2] = {(unsigned)(g * CH_ + (r0 + 32 * w + c) * 16), (unsigned)((g + 4) * CH_ + (r0 + 32 * w + c) * 16)}, so[NT];
  pin(voffA[0]); pin(voffA[1]);
#pragma unroll
  for (int t = 0; t < NT; ++t) { so[t] = p2_tile(t, tn) * KT_; asm volatile("" : "+s"(so[t])); }
  bf16x8 xr[LEAD + 1][2][2];
  auto ldx = [&](int t, int q) {
    if (q & 1) ldb_dev<256>(xr[t % (LEAD + 1)][q >> 1][1], voffA[q >> 1], srdA, so[t]); else ldb_dev<0>(xr[t % (LEAD + 1)][q >> 1][0], voffA[q >> 1], srdA, so[t]);
  };
  f32x4 acc[2][4], accm[2];
  auto mm = [&](const P2Frag& f, const bf16x8 (&x)[2], auto i_c) {
    constexpr int i = decltype(i_c)::v, j = i >> 1, m = i & 1;
    if constexpr (i < 8) mfma_ip(acc[m][j], cat8(f.w[j][0], f.w[j][1]), x[m]);
    else mfma_ip(accm[m], f.wb, x[m]);
  };
  P2Frag fa, fb;
  char* ocp = (char*)OC + (size_t)(tn * 8 + 2 * (g & 1) + (g >> 1)) * CH_ + (r0 + 32 * w + c) * 16;
  char* op = (char*)O + blockIdx.x * 16384 + w * 4096 + lane * 16;
  float* qp = ssq + tn * S_ + r0 + 32 * w + lane;
  const u64* fp = flags + c * FS;
  pin(ocp); pin(op); pin(qp); pin(fp);
  wait_vm<4>();
  TLS(1, 3);
  TLS(2, 0);
  bar();
  sfor<0, 9>([&](auto r) { rd(fa, IC<0>{}, 0, r); });
  asm volatile("s_waitcnt vmcnt(0)" : "+v"(res[0][0]), "+v"(res[0][1]), "+v"(res[1][0]), "+v"(res[1][1]) :: "memory");
#pragma unroll
  for (int m = 0; m < 2; ++m) {
    accm[m] = (f32x4){xm[m], 0, 0, 0}; pin(accm[m]);
#pragma unroll
    for (int jp = 0; jp < 2; ++jp) {
      auto e0 = __builtin_amdgcn_permlane16_swap(res[m][jp][0], res[m][jp][2], false, false);
      auto e1 = __builtin_amdgcn_permlane16_swap(res[m][jp][1], res[m][jp][3], false, false);
#pragma unroll
      for (int j = 0; j < 2; ++j) { acc[m][2 * jp + j] = (f32x4){bf_lo(e0[j]), bf_hi(e0[j]), bf_lo(e1[j]), bf_hi(e1[j])}; pin(acc[m][2 * jp + j]); }
    }
  }
  PAGE(7, p20);
  SB();
  const unsigned own = (0x5555u << (tn & 1)) & ~(1u << tn), sib = 0x5555u << ((tn & 1) ^ 1);
  Poll pv; poll_regs(pv);
  if (w == 0) poll_flags(pv, fp, own, tok);
  bar();
  acquire_agent();
  u64 fv;
  constexpr int NF = P2_NF;
#pragma unroll
  for (int q = 0; q < 4 * LEAD; ++q) { if (q == NF) asm volatile("global_load_dwordx2 %0, %1, off sc1" : "=v"(fv) : "v"(fp)); ldx(q >> 2, q & 3); }
  TLS(2, 1);
  mfma_fence(); SB();
  sfor<0, NT>([&](auto t_c) {
    constexpr int t = decltype(t_c)::v, more = t + LEAD < NT, sent = 4 * (t + LEAD < NT ? t + LEAD : NT);
    bf16x8 (&x)[2][2] = xr[t % (LEAD + 1)];
    if constexpr (t == 8 - LEAD) {
      static_assert(4 * t + 2 > NF, "");
      asm volatile("s_waitcnt vmcnt(%3)" : "+v"(x[0][0]), "+v"(x[0][1]), "+v"(fv) : "n"(sent - 4 * t - 2) : "memory");
      settle_flags(fv, fp, sib, tok);
    } else asm volatile("s_waitcnt vmcnt(%2)" : "+v"(x[0][0]), "+v"(x[0][1]) : "n"(sent - 4 * t - 2 + (4 * t + 2 <= NF)) : "memory");
    if constexpr (t == 0) poll_done(pv);
    sfor<0, 10>([&](auto i) {
      constexpr int ii = decltype(i)::v;
      mm(fa, x[0], i);
      if constexpr (ii < 9) rd(fb, t_c, 1, i);
      if constexpr (more && (ii == 2 || ii == 6)) ldx(t + LEAD, ii == 6);
      SB();
    });
    if constexpr (t == 7) PAGE(8, r1); else if constexpr (t == 14) PAGE(20, r1);
    if constexpr (t == 7) bar();
    if constexpr (t == P2_TB) bar();
    asm volatile("s_waitcnt vmcnt(%2)" : "+v"(x[1][0]), "+v"(x[1][1]) : "n"(sent + 2 * more - 4 * t - 4 + (4 * t + 4 <= NF)) : "memory");
    sfor<0, 10>([&](auto i) {
      constexpr int ii = decltype(i)::v;
      mm(fb, x[1], i);
      if constexpr (t < NT - 1 && ii < 9) rd(fa, IC<t + 1>{}, 0, i);
      if constexpr (more && (ii == 2 || ii == 6)) ldx(t + LEAD, 2 + (ii == 6));
      SB();
    });
  });
  mfma_fence(); SB();
  TLS(2, 2);
  auto out8 = [&](f32x4 v0, f32x4 v1) {
    const u32x2 a0 = pk4(v0), a1 = pk4(v1);
    auto e0 = __builtin_amdgcn_permlane16_swap(a0[0], a1[0], false, false);
    auto e1 = __builtin_amdgcn_permlane16_swap(a0[1], a1[1], false, false);
    return (u32x4){e0[0], e1[0], e0[1], e1[1]};
  };
  float qs[2];
#pragma unroll
  for (int m = 0; m < 2; ++m) {
    const float mean = accm[m][0] + accm[m][1];
    f32x4 q = {0, 0, 0, 0};
#pragma unroll
    for (int jp = 0; jp < 2; ++jp) {
      const f32x4 d0 = acc[m][2 * jp] - mean, d1 = acc[m][2 * jp + 1] - mean;
      st_wt(ocp + jp * (4 * CH_) + m * 256, out8(d0, d1));
      q += d0 * d0; q += d1 * d1;
    }
    qs[m] = red32_add(red16_add((q[0] + q[1]) + (q[2] + q[3])));
  }
  if (lane < 32) st_wt(qp, g ? qs[1] : qs[0]);
  SB();
#pragma unroll
  for (int m = 0; m < 2; ++m)
#pragma unroll
    for (int jp = 0; jp < 2; ++jp) st_plain(op + m * 2048 + jp * 1024, out8(acc[m][2 * jp], acc[m][2 * jp + 1]));
  TLS(2, 3);
}

__global__ __launch_bounds__(512, 2) void km_block(const u16* __restrict__ x, const u16* __restrict__ wqkv, const u16* __restrict__ wo,
                                                   const u16* __restrict__ wg, const u16* __restrict__ wu, const u16* __restrict__ wd,
                                                   u16* __restrict__ out, char* __restrict__ ws) {
  __shared__ __attribute__((aligned(16))) char smem_[KM_LDS];
  LAS char* smem = (LAS char*)smem_;
  const unsigned lds0 = (unsigned)(size_t)smem;
  const int tid = threadIdx.x, lane = tid & 63, w = __builtin_amdgcn_readfirstlane(tid >> 6);
  const int bid = blockIdx.x, xcd = bid & 7, idx = bid >> 3;
  const int tm = 4 * (xcd >> 1) + (idx & 3), tn = 2 * (idx >> 2) + (xcd & 1);

  const int r0 = tm * 128;
  const int tm3 = 4 * (xcd >> 2) + (idx & 3), tn3 = 8 * (xcd & 3) + (idx >> 2);
  const int pm = 8 * (tm >> 3) + 2 * (tm & 3) + ((tm >> 2) & 1);
  u16* const hc = (u16*)(ws + WS_HC); u16* const o = (u16*)(ws + WS_O); u16* const h = (u16*)(ws + WS_H);
  float* const ssq = (float*)(ws + WS_SSQ); u16* const act = (u16*)(ws + WS_ACT);
  u64* const flg = (u64*)(ws + WS_FLG);
  u32x4* const part = (u32x4*)(ws + WS_PART);
  const int w4 = w & 3;
  const int lr = lane >> 3, lc = lane & 7;
  const int g = lane >> 4, c = lane & 15;
  TLS(1, 0);
  auto p3_voffB = [&](int j, int lane) {
    const int ch = (w4 + 4 * j) * 64 + lane, gr = ch / 120, r = ch - gr * 120, rr = r / 14, pos = r - rr * 14;
    return r < 112 ? (unsigned)((8 * gr + rr) * (F_ * 2) + (pos << 4)) : OOB;
  };
  const unsigned sB3 = (unsigned)tn3 * 224u;
#define WD_COPY() const int cd = (3 - (int)((size_t)wd >> 7)) & 7; char* const wdc = (char*)(((size_t)ws + WS_WDC + 1023) & ~(size_t)1023) + ((4 - cd) & 7) * 128
  constexpr int NY = 14, NX = 24, NS4 = 32;
  const int kh = tn & 1;
  const int t40 = (pm & 1) * 28 + NY - 1 + kh, dir = 2 * kh - 1;
  auto tile4 = [&](int s) { return (unsigned)(t40 + dir * s); };
#define P4_DEFS() WD_COPY(); \
    const i32x4 srdW = make_srd((tn & 7) == cd ? wdc : (const char*)wd, F_ * D_ * 2), srdW1 = make_srd(((tn ^ 1) & 7) == cd ? wdc : (const char*)wd, F_ * D_ * 2); \
    const unsigned voffW0 = (unsigned)((8 * w4 + lr) * (D_ * 2) + (((((lc >> 1) ^ (((lane >> 4) & 1) | ((w4 & 1) << 1))) << 1) | (lc & 1)) << 4)); \
    auto issueW = [&](unsigned T, int slot, bool both) { \
      const unsigned lb = lds0 + P4_W + slot * 16384 + w4 * 1024, so = T * (64 * D_ * 2); \
      _Pragma("unroll") for (int j = 0; j < 2; ++j) { \
        dma16_dev(lb + j * 4096, voffW0, srdW, so + j * (32 * D_ * 2) + tn * 128); \
        if (both) dma16_dev(lb + 8192 + j * 4096, voffW0, srdW1, so + j * (32 * D_ * 2) + (tn ^ 1) * 128); \
      } \
    }; (void)issueW
  u64 tok; unsigned tag;
  auto set_tok = [&](u32x2 ep) {
    const u64 e = ((u64)(unsigned)__builtin_amdgcn_readfirstlane(ep[1]) << 32) | (unsigned)__builtin_amdgcn_readfirstlane(ep[0]);
    tok = e * 6364136223846793005ull + 1442695040888963407ull;
    tag = (unsigned)(tok >> 32);
  };

  const i32x4 srdX = make_srd(x, S_ * D_ * 2), srdQ = make_srd(wqkv, D_ * 3 * D_ * 2);
  unsigned voffX = 0, voffQ = 0;
  auto p1_offs = [&]() {
    LANE_IDS();
    voffX = (unsigned)((8 * w4 + lr) * (D_ * 2) + ((lc ^ ((4 * w4 + (lane >> 4)) & 7)) << 4));
    voffQ = (unsigned)((8 * w4 + lr) * (3 * D_ * 2) + (((((lc >> 1) ^ (((lane >> 4) & 1) | ((w4 & 1) << 1))) << 1) | (lc & 1)) << 4));
  };
  const u64 halo = w4 == 1 ? ~0ull : (w4 == 0 ? ~0xffull : 0ull);
  const int rX = r0 >= 16 ? r0 - 16 : 0;
  auto issueX = [&](int t, int slot) {
    const unsigned lb = lds0 + slot * K1_AS + w4 * 1024, sA = (unsigned)r0 * (D_ * 2) + t * 128;
#pragma unroll
    for (int j = 0; j < 4; ++j) dma16(lb + j * 4096, voffX, srdX, sA + j * (32 * D_ * 2));
    dma16_lanes(halo, lb + 16384, voffX, srdX, (unsigned)rX * (D_ * 2) + t * 128);
  };
  auto issueQ = [&](int t) {
    const unsigned lb = lds0 + K1_W0 + (t & 3) * K1_WS + w4 * 1024, sW = (unsigned)tn * 128 + (unsigned)(t * 64) * (3 * D_ * 2);
#pragma unroll
    for (int s = 0; s < 3; ++s)
#pragma unroll
      for (int j = 0; j < 2; ++j) dma16(lb + s * 8192 + j * 4096, voffQ, srdQ, sW + (32 * j) * (3 * D_ * 2) + s * (D_ * 2));
  };
  {
    const int cq = (3 - (int)((size_t)wqkv >> 7)) & 7;
    dma16_dev_if0(w | ((tn ^ cq) & 1), lds0 + K1_DUMMY, (unsigned)lane * 2048u, srdQ, (unsigned)((idx >> 1) * 64) * 2048u + (cq + 8 * (idx & 1)) * 128u);
  }
  dma16_dev_if0(w - 3, lds0 + K1_DUMMY, (unsigned)lane * 4096u, code_srd(), (unsigned)idx * 128u);
  if (w >= 4) {
    p1_offs();
    issueQ(0);
#pragma clang loop unroll(disable)
    for (int t = 0, slot = 0; t < 12; ++t) {
      if (t >= 2) { wait_lgkm0(); wait_vm<16>(); bar(); }
      issueX(t, slot); issueQ(t + 1);
      if (t == 1 && w4 == 0 && lane < 28) {
        const unsigned one = lane < 24 ? 0x3f803f80u : 0u;
        *(LAS u32x4*)(smem + (lane < 24 ? (lane >> 3) * K1_AS + 16384 + (lane & 7) * 16 : L_SYNC + (lane & 3) * 16)) = (u32x4){one, one, one, one};
      }
      slot = slot == 2 ? 0 : slot + 1;
    }
  }
  asm volatile("" ::: "memory");
  if (w >= 4) {
    PAD(6, p9);
    wait_vm<16>(); bar();
    issueX(12, 0); issueQ(13);
    SB();
    const int lw = w4;
    LANE_IDS();
    const i32x4 srdWo = make_srd(wo, D_ * D_ * 2);
    const unsigned voffWo = (unsigned)((8 * lw + lr) * (D_ * 2) + (((((lc >> 1) ^ (((lane >> 4) & 1) | ((lw & 1) << 1))) << 1) | (lc & 1)) << 4));
    auto p2W = [&](unsigned tile, unsigned dst) {
#pragma unroll
      for (int j = 0; j < 2; ++j) dma16(lds0 + lw * 1024 + dst + j * 4096, voffWo, srdWo, (unsigned)tn * 128 + (tile * 64 + 32 * j) * (D_ * 2));
    };
    WD_COPY();
    const int cl = bid * 28 + lw * 7 + lr;
    const unsigned coff = (unsigned)((cl >> 1) * (D_ * 2) + (cd + 8 * (cl & 1)) * 128 + lc * 16);
    u32x4 wc;
    {
      constexpr int NT = D_ / 64;
      const int e = 36 * lw + (lane < 36 ? lane : 0);
      const char* const sp = ws + WS_STAT + (e < 128 ? r0 + e : rX + e - 128) * 16;
      const u16* const wrow = wo + (size_t)(bid * 4 + lw) * D_ + lane * 8;
      u32x4 sv, wa, wb; u32x2 ep;
      static_assert(T_WO >= 9, "");
      sfor<10, NT - 3>([&](auto t_c) {
        constexpr int t = decltype(t_c)::v;
        wait_vm<(t == T_WO + 2 ? 20 : (t == T_WO + 1 ? 19 : 16))>();
        bar();
        if constexpr (t == NT - 5) ld_async_dev(sv, sp);
        issueX(t + 3, t % 3);
        if constexpr (t + 4 < NT) issueQ(t + 4);
        if constexpr (t == T_WO) { ld_async(wa, wrow); ld_async(wb, wrow + 512); ld_async_dev(ep, flg); }
        if constexpr (t == T_WO + 2) {
          ldb_async<0>(wc, lane < 56 ? coff : OOB, make_srd(wd, D_ * F_ * 2), 0);
          const int co = (3 - (int)((size_t)wo >> 7)) & 7;
          dma16(lds0 + K1_DUMMY, lane < 16 && ((tn ^ co) & 1) == 0 ? (unsigned)lane * 1024u : OOB, srdWo, (unsigned)(idx * 4 + lw) * 16384u + co * 128u);
          asm volatile("s_waitcnt vmcnt(17)" : "+v"(wa), "+v"(wb), "+v"(ep) :: "memory");
          set_tok(ep);
          float v = 0.f;
#pragma unroll
          for (int i = 0; i < 4; ++i) v += (bf_lo(wa[i]) + bf_hi(wa[i])) + (bf_lo(wb[i]) + bf_hi(wb[i]));
          v = wave_sum(v) * (1.f / D_);
          const float hi = rbf(v);
          if (lane == 0) st_wt(ws + WS_WBAR + (bid * 4 + lw) * 8, (u32x2){pk_bf16(hi, v - hi), tag});
        }
      });
      asm volatile("s_waitcnt vmcnt(13)" : "+v"(sv) :: "memory");
      PAGE(9, r1);
      bar();
#pragma unroll
      for (int s = 0; s < 4; ++s) p2W(p2_tile(s, tn), P2_W2 + s * 8192);
      settle(sv, sp, tag);
      LAS float* const tb = (LAS float*)(smem + K1_STT) + e;
      tb[0] = -rbf(__uint_as_float(sv[0])); tb[144] = __uint_as_float(sv[2]); tb[288] = __uint_as_float(sv[0]);
    }
    dma16(lds0 + K1_DUMMY, lane < 56 ? (unsigned)lane * 128u : OOB, make_srd((lw & 1) ? wu : wg, D_ * F_ * 2), (unsigned)bid * 14336u + (lw >> 1) * 7168u);
    wait_lgkm0(); wait_vm<12>(); bar();
#pragma unroll
    for (int i = 0; i < 2; ++i) {
      const LAS float* const rs = (const LAS float*)(smem + K1_STT) + 144;
      const int b = 32 * lw + 16 * i, k1 = b + 4 * g + (c & 1), k0 = b ? k1 - 16 : k1 + 128;
      *(LAS f32x4*)(smem + K1_SC + (2 * lw + i) * 1024 + lane * 16) = (f32x4){rs[k0], rs[k0 + 2], rs[k1], rs[k1 + 2]} * (rs[b + c] * SL2E);
    }
    wait_lgkm0();
    bar();
    {
      const char* const bp = ws + WS_WBAR + (256 * lw + 2 * lane) * 8;
      u32x4 b0, b1;
      asm volatile("s_waitcnt vmcnt(11)" : "+v"(wc) :: "memory");
      st_wt_lanes((1ull << 56) - 1, wdc + coff, wc);
      ld_async_dev(b0, bp); ld_async_dev(b1, bp + 1024);
      dma16_dev_if0(lw - 3, lds0 + K1_DUMMY, (unsigned)lane * 4096u, code_srd(), (unsigned)idx * 128u);
      bar();
#pragma unroll
      for (int s = 0; s < 4; ++s) p2W(p2_tile(s + 4, tn), P2_W + s * 8192);
      asm volatile("s_waitcnt vmcnt(8)" : "+v"(b0), "+v"(b1) :: "memory");
      settle(b0, bp, tag); settle(b1, bp + 1024, tag);
      const int s0 = (2 * lw - (tn >> 1) - 1) & 7, hi = (((lane >> 5) ^ tn) & 1) * 256 + (lane & 31);
      LAS unsigned* const wt = (LAS unsigned*)(smem + P2_WB) + s0 * 32 + hi, * const wu_ = (LAS unsigned*)(smem + P2_WB) + ((s0 + 1) & 7) * 32 + hi;
      wt[0] = __builtin_amdgcn_perm(b0[2], b0[0], 0x05040100u); wt[512] = __builtin_amdgcn_perm(b0[2], b0[0], 0x07060302u);
      wu_[0] = __builtin_amdgcn_perm(b1[2], b1[0], 0x05040100u); wu_[512] = __builtin_amdgcn_perm(b1[2], b1[0], 0x07060302u);
    }
    wait_vm<0>(); wait_lgkm0(); bar();
    if (lw == 0) signal_flag(flg + FL_1 + (tm * 16 + tn) * FS, tok);
#pragma unroll
    for (int s = 8; s < 16; ++s) p2W(p2_tile(s, tn), (s - 8) * 8192);
    bar();
    unsigned vo3[4];
#pragma unroll
    for (int j = 0; j < 4; ++j) { vo3[j] = p3_voffB(j, lane); pin(vo3[j]); }
    VISIT4(lw, 10, 11, 12, 13, r2);
    wait_vm<0>(); bar();
    bar();
#pragma unroll
    for (int i = 0; i < 3; ++i) {
      const i32x4 srd = make_srd(i == 1 ? wu : wg, D_ * F_ * 2);
      const unsigned lb = lds0 + K3_B0 + (i == 1 ? K3_UOFF : (i == 2 ? K3_BB : 0)) + lw * 1024, so = sB3 + (i == 2 ? 64 * F_ * 2 : 0);
#pragma unroll
      for (int j = 0; j < 3; ++j) dma16(lb + j * 4096, vo3[j], srd, so);
      if (lw < 3) dma16(lb + 3 * 4096, vo3[3], srd, so);
    }
    dma16_dev_if0(lw - 3, lds0 + K1_DUMMY, (unsigned)lane * 4096u, code_srd(), (unsigned)idx * 128u);
    wait_vm<4>();
  } else {
    constexpr int NT = D_ / 64;
    const int srow = (xcd * 32 + idx) * 8 + 2 * w;
    const u16* const xrow = x + (size_t)srow * D_ + lane * 8;
    u32x4 xv[4]; u32x2 ep;
    ld_async_dev(ep, flg);
#pragma unroll
    for (int i = 0; i < 4; ++i) ld_async(xv[i], xrow + 512 * i);
    const int when = __builtin_amdgcn_readfirstlane(w == 3 ? 0 : w);
    asm volatile(".Lkm_first:");
    const i32x4 srdT = w < 3 ? make_srd(wqkv, D_ * 3 * D_ * 2) : code_srd();
    const unsigned voffT = (unsigned)lane * (w < 3 ? 128u : 4096u), soffT = w < 3 ? (unsigned)bid * 8192u + w * (D_ * D_ * 2) : (unsigned)idx * 128u;
    const int cq = (3 - (int)((size_t)wqkv >> 7)) & 7;
    auto touch = [&](int now) {
      dma16_dev_if0((when - now) | ((tn ^ cq) & 1) | (w == 3), lds0 + K1_DUMMY, (unsigned)lane * 2048u, srdQ, (unsigned)(1024 * w + (idx >> 1) * 64) * 2048u + (cq + 8 * (idx & 1)) * 128u);
      dma16_dev_if0(when - now, lds0 + K1_DUMMY, voffT, srdT, soffT);
    };
    dma16_dev(lds0 + K1_DUMMY, OOB, srdQ, 0);
    dma16_dev(lds0 + K1_DUMMY, w ? OOB : voffT, srdT, soffT);
    unsigned tl0 = 0;
    if (w == 1) {
      const i32x4 sb = make_srd(idx < 8 ? wg : idx < 16 ? wu : idx < 24 ? wd : idx < 28 ? out : wo, idx < 24 ? D_ * F_ * 2 : idx < 28 ? S_ * D_ * 2 : D_ * D_ * 2);
      asm volatile("s_buffer_load_dword %0, %1, %2\n\ts_buffer_load_dword %0, %3, %4\n\ts_waitcnt lgkmcnt(0)" : "+s"(tl0)
                   : "s"(sb), "s"((unsigned)(idx < 24 ? idx & 7 : idx & 3) << (idx < 28 ? 20 : 19)), "s"(make_srd(ws, 64u << 20)), "s"((unsigned)idx << 21));
    }
    const int hh = w & 1, rh = w >> 1;
    const int sw = (lane >> 1) & 7;
    const int aoff0 = (64 * rh + c) * 128 + ((g ^ sw) << 4), hoff0 = (128 + c) * 128 + ((g ^ sw) << 4);
    const int q4 = c >> 2, p4 = lane & 3, fR = ((q4 >> 1) & 1) | ((g & 1) << 1);
    const int woff0 = K1_W0 + (8 * g + q4) * 128 + (((2 * hh + rh) ^ fR) << 5) + 8 * p4;
    bf16x8 fx[5][2]; s16x4 fw[2][3][2][2];
    auto rdx = [&](int t, int m, int ks) { fx[m][ks] = lds_b128(smem + (t % 3) * K1_AS + ((m < 4 ? aoff0 + m * 2048 : hoff0) ^ (ks << 6))); };
    auto rdw = [&](int t, int sl, int mat, int q) {
      fw[sl][mat][q >> 1][q & 1] = lds_tr(smem + (t & 3) * K1_WS + (woff0 ^ (sl << 5)) + (q >> 1) * 4096 + mat * 8192 + (q & 1) * 512);
    };
    f32x4 aq[4][2], ak[4][2], av[4][2], ahq, ahk, ahv;
#pragma unroll
    for (int m = 0; m < 4; ++m)
#pragma unroll
      for (int s = 0; s < 2; ++s) { zero_pin(aq[m][s]); zero_pin(ak[m][s]); zero_pin(av[m][s]); }
    zero_pin(ahq); zero_pin(ahk); zero_pin(ahv);
    auto mm = [&](auto h_c, auto i_c) {
      constexpr int sl = decltype(h_c)::v, i = decltype(i_c)::v - (sl ? 0 : 6), pr = (i + 6) >> 1, ks = (i ^ pr) & 1;
      if constexpr (i < 0) {
        if constexpr (pr == 0) mfma_ip(ahk, cat8(fw[0][1][ks][0], fw[0][1][ks][1]), fx[4][ks]);
        else if constexpr (pr == 1) mfma_ip(ahv, fx[4][ks], cat8(fw[0][2][ks][0], fw[0][2][ks][1]));
        else mfma_ip(ahq, cat8(fw[0][0][ks][0], fw[0][0][ks][1]), fx[4][ks]);
      } else {
        constexpr int m = i / 6, u = (i % 6) >> 1, mat = (m & 1) ? 2 - u : u;
        if constexpr (mat == 0) mfma_ip(aq[m][sl], cat8(fw[sl][0][ks][0], fw[sl][0][ks][1]), fx[m][ks]);
        else if constexpr (mat == 1) mfma_ip(ak[m][sl], cat8(fw[sl][1][ks][0], fw[sl][1][ks][1]), fx[m][ks]);
        else mfma_ip(av[m][sl], fx[m][ks], cat8(fw[sl][2][ks][0], fw[sl][2][ks][1]));
      }
    };
    auto rdn = [&](int t, auto r_c) {
      constexpr int r = decltype(r_c)::v;
      if constexpr (r < 2) rdx(t, 4, r);
      else if constexpr (r < 14) rdw(t, 0, (((r - 2) >> 2) + 1) % 3, (r - 2) & 3);
      else rdx(t, (r - 14) >> 1, r & 1);
    };
    VISIT4(w, 1, 2, 3, 4, r1);
    {
      asm volatile("s_waitcnt vmcnt(2)" : "+v"(xv[0]), "+v"(xv[1]), "+v"(xv[2]), "+v"(xv[3]), "+v"(ep) :: "memory");
      set_tok(ep);
      float a[2] = {0.f, 0.f}, q[2] = {0.f, 0.f};
      const bf16x2 one2 = __builtin_bit_cast(bf16x2, 0x3f803f80u);
#pragma unroll
      for (int d = 0; d < 8; ++d)
#pragma unroll
        for (int r = 0; r < 2; ++r) {
          const unsigned uw = xv[2 * r + (d >> 2)][d & 3];
          const bf16x2 u = __builtin_bit_cast(bf16x2, uw);
          a[r] = __builtin_amdgcn_fdot2_f32_bf16(u, one2, a[r], false); q[r] = __builtin_amdgcn_fdot2_f32_bf16(u, u, q[r], false);
        }
      auto ta = __builtin_amdgcn_permlane32_swap(__float_as_uint(a[0]), __float_as_uint(a[1]), false, false);
      auto tq = __builtin_amdgcn_permlane32_swap(__float_as_uint(q[0]), __float_as_uint(q[1]), false, false);
      auto tv = __builtin_amdgcn_permlane16_swap(__float_as_uint(__uint_as_float(ta[0]) + __uint_as_float(ta[1])), __float_as_uint(__uint_as_float(tq[0]) + __uint_as_float(tq[1])), false, false);
      float v = __uint_as_float(tv[0]) + __uint_as_float(tv[1]);
      v += __builtin_bit_cast(float, __builtin_amdgcn_update_dpp(0, __builtin_bit_cast(int, v), 0xB1, 0xf, 0xf, true));
      v += __builtin_bit_cast(float, __builtin_amdgcn_update_dpp(0, __builtin_bit_cast(int, v), 0x4E, 0xf, 0xf, true));
      v += __builtin_bit_cast(float, __builtin_amdgcn_update_dpp(0, __builtin_bit_cast(int, v), 0x141, 0xf, 0xf, true));
      v += __builtin_bit_cast(float, __builtin_amdgcn_update_dpp(0, __builtin_bit_cast(int, v), 0x140, 0xf, 0xf, true));
      auto tm_ = __builtin_amdgcn_permlane16_swap(__float_as_uint(v), __float_as_uint(v), false, false);
      const float mean = __uint_as_float(tm_[0]) * (1.f / D_);
      const float rs = ln_rstd(fmaxf(fmaf(-mean, mean, __uint_as_float(tm_[1]) * (1.f / D_)), 0.f));
      st_wt_lanes(0x100000001ull, ws + WS_STAT + (srow + (lane >> 5)) * 16, (u32x4){__float_as_uint(mean), tag, __float_as_uint(rs), tag});
    }
    const int d1 = (c & 14) - 4 * g;
    const float NINF = -__builtin_inff();
    float b0a = d1 + 16 <= 14 ? 0.f : NINF, b0b = d1 + 14 <= 14 ? 0.f : NINF, b1a = d1 >= 0 ? 0.f : NINF, b1b = d1 - 2 >= 0 ? 0.f : NINF;
    unsigned selA = (c & 1) ? 0x01000c0cu : 0x0c0c0100u, selB = (c & 1) ? 0x03020c0cu : 0x0c0c0302u;
    char* ost = (char*)o + (size_t)(tn * 8 + 4 * hh + 2 * ((g & 1) ^ rh) + (g >> 1)) * CH_ + (r0 + 64 * rh + c) * 16;
    pin(b0a); pin(b0b); pin(b1a); pin(b1b); pin(selA); pin(selB); pin(ost);
    __builtin_amdgcn_s_setprio(1);
    bar();
    TLS(1, 1);
    sfor<0, 20>([&](auto r) { rdn(0, r); });
    mfma_fence(); SB();
    constexpr int NB = 3;
    auto fill = [&](auto t_c, auto h_c, auto i_c) {
      constexpr int t = decltype(t_c)::v, hf = decltype(h_c)::v, i = decltype(i_c)::v;
      if constexpr (t == 2 && hf == 0 && i == 14) touch(2);
      if constexpr (t == 2 && hf == 0 && i == 18) dma16_dev_if0(w - 3, lds0 + K1_DUMMY, (unsigned)lane * (256 * FS * 8), make_srd(flg + FL_1, 4 * 256 * FS * 8), (unsigned)bid * (FS * 8));
    };
    unsigned cw;
    float nmc[5], csv[2], xm[2];
    f32x4 nm4[5], rs4[5], csq[2], csk[2], sc[4];
    sfor<0, NT>([&](auto t_c) {
      constexpr int t = decltype(t_c)::v;
      constexpr bool last = t == NT - 1;
      if constexpr (t == 0) { touch(1); SB(); }
      const int l2 = last ? fresh_lane() : 0, g2 = l2 >> 4, c2 = l2 & 15;
      const LAS float* const st = (const LAS float*)(smem + K1_STT);
      const LAS float* const ct = (const LAS float*)(smem + K1_CST) + hh * 96;
      sfor<0, 30>([&](auto i_c) {
        constexpr int i = decltype(i_c)::v;
        mm(IC<0>{}, i_c);
        if constexpr (i < 2) rdx(t, 3, i);
        else if constexpr (i < 14) rdw(t, 1, (i - 2) >> 2, (i - 2) & 3);
        if constexpr (last) {
          if constexpr (i == 14) { cw = lds0 + K1_CST + hh * 384 + rh * 64 + l2; pin(cw); }
          if constexpr (i == 15)
            asm volatile("s_mov_b32 exec_lo, 0x10001\n\ts_mov_b32 exec_hi, 0x10001\n\tds_write_b128 %0, %1\n\tds_write_b128 %0, %2 offset:128\n\ts_mov_b64 exec, -1"
                         :: "v"(cw), "v"(ahq), "v"(ahk) : "memory");
          if constexpr (i == 16) { cw = lds0 + K1_CST + hh * 384 + rh * 64 + 4 * l2; pin(cw); }
          if constexpr (i == 17)
            asm volatile("s_mov_b64 exec, 0xffff\n\tds_write_b32 %0, %1 offset:256\n\ts_mov_b64 exec, -1" :: "v"(cw), "v"(ahv[0]) : "memory");
          if constexpr (i >= 18 && i < 23) { constexpr int j = i - 18; const int r = j < 4 ? 64 * rh + 16 * j + c2 : 128 + c2; nmc[j] = st[r]; }
          if constexpr (i == 23) { xm[0] = st[288 + 32 * w + c2]; xm[1] = st[288 + 32 * w + 16 + c2]; }
        } else fill(t_c, IC<0>{}, i_c);
        SB();
      });
      sfor<0, NB>([&](auto i) { mm(IC<1>{}, i); fill(t_c, IC<1>{}, i); SB(); });
      if constexpr (t == 0) PAGE(1, p5); else if constexpr (t == 4) PAGE(2, p6); else if constexpr (t == 9) PAGE(3, p7); else if constexpr (t == 14) PAGE(4, p8);
      wait_lgkm0(); bar(); SB();
      sfor<NB, 24>([&](auto i_c) {
        constexpr int j = decltype(i_c)::v - NB;
        mm(IC<1>{}, i_c);
        if constexpr (last) {
          if constexpr (j < 2) { const int b = 16 * (j ^ rh); csk[j] = *(const LAS f32x4*)(ct + 32 + b + 4 * g2); csv[j] = ct[64 + b + c2]; }
          else if constexpr (j < 4) csq[j - 2] = *(const LAS f32x4*)(ct + 16 * ((j - 2) ^ rh) + 4 * g2);
          else if constexpr (j < 14) {
            constexpr int q = (j - 4) >> 1;
            const int r = q < 4 ? 64 * rh + 16 * q + 4 * g2 : 128 + 4 * g2;
            if constexpr (j & 1) rs4[q] = *(const LAS f32x4*)(st + 144 + r); else nm4[q] = *(const LAS f32x4*)(st + r);
          } else if constexpr (j < 18) sc[j - 14] = *(const LAS f32x4*)(smem + K1_SC + rh * 4096 + (j - 14) * 1024 + l2 * 16);
        } else {
          if constexpr (j < 20) rdn(t + 1, IC<j>{});
          fill(t_c, IC<1>{}, i_c);
        }
        SB();
      });
    });
    mfma_fence(); SB();
    TLS(1, 2);
    {
    const int lane = fresh_lane();
    LAS u32x2* const ex = (LAS u32x2*)(smem + K1_EX + lane * 16);
    u32x2 qp[4][2], kp[5][2], vp[5][2];
#pragma unroll
    for (int s = 0; s < 2; ++s) { kp[4][s] = pk4(ak[3][s] + nmc[3] * csk[s]); vp[4][s] = pk4((av[3][s] + nm4[3] * csv[s]) * rs4[3]); }
    ex[hh * 512 + rh] = pk4(ahk + nmc[4] * csk[0]); ex[hh * 512 + 128 + rh] = pk4((ahv + nm4[4] * csv[0]) * rs4[4]);
    volatile LAS u32x2* const exw = ex + (rh ? 1024 : hh * 512 + 256);
    exw[1] = kp[4][0]; exw[0] = kp[4][1]; exw[129] = vp[4][0]; exw[128] = vp[4][1];
    SB();
#pragma unroll
    for (int m = 0; m < 4; ++m)
#pragma unroll
      for (int s = 0; s < 2; ++s) {
        qp[m][s] = pk4(aq[m][s] + nmc[m] * csq[s]);
        if (m < 3) kp[m + 1][s] = pk4(ak[m][s] + nmc[m] * csk[s]);
      }
    TLR(a);
    PAGE(5, r1);
    wait_lgkm0(); bar();
    TLR(b);
    const u32x4 kpv = *(const LAS u32x4*)(ex + hh * 512 + rh * 256), vpv = *(const LAS u32x4*)(ex + hh * 512 + rh * 256 + 128);
    kp[0][0] = (u32x2){kpv[0], kpv[1]}; kp[0][1] = (u32x2){kpv[2], kpv[3]}; vp[0][0] = (u32x2){vpv[0], vpv[1]}; vp[0][1] = (u32x2){vpv[2], vpv[3]};
    f32x4 s0[4], s1[4];
    const f32x4 zero4 = {0, 0, 0, 0};
#pragma unroll
    for (int m = 3; m >= 0; --m) {
      const bf16x8 qf = cat8u(qp[m][0], qp[m][1]);
      s1[m] = mfma(cat8u(kp[m + 1][0], kp[m + 1][1]), qf, zero4);
      s0[m] = mfma(cat8u(kp[m][0], kp[m][1]), qf, zero4);
    }
#pragma unroll
    for (int m = 0; m < 3; ++m)
#pragma unroll
      for (int s = 0; s < 2; ++s) vp[m + 1][s] = pk4((av[m][s] + nm4[m] * csv[s]) * rs4[m]);
    SB();
    const bool first = (r0 + 64 * rh) == 0;
    const bf16x8 ones = __builtin_bit_cast(bf16x8, (u32x4){0x3f803f80u, 0x3f803f80u, 0x3f803f80u, 0x3f803f80u});
    auto par = [&](float e, float o_) { return __builtin_amdgcn_inverse_ballot_w64(0xaaaaaaaaaaaaaaaaull) ? o_ : e; };
    float l[4][4], mx[4];
#pragma unroll
    for (int m = 3; m >= 0; --m) {
      l[m][0] = fmaf(par(s0[m][0], s0[m][1]), sc[m][0], (m == 0 && first) ? NINF : b0a);
      l[m][1] = fmaf(par(s0[m][2], s0[m][3]), sc[m][1], (m == 0 && first) ? NINF : b0b);
      l[m][2] = fmaf(par(s1[m][0], s1[m][1]), sc[m][2], b1a);
      l[m][3] = fmaf(par(s1[m][2], s1[m][3]), sc[m][3], b1b);
      mx[m] = vmax(vmax3(l[m][0], l[m][1], l[m][2]), l[m][3]);
    }
    asm("s_nop 1\n\tv_permlane16_swap_b32 %0, %1\n\tv_permlane16_swap_b32 %2, %3\n\tv_max_f32 %0, %0, %1\n\tv_max_f32 %2, %2, %3\n\ts_nop 1\n\t"
        "v_permlane32_swap_b32 %0, %2\n\tv_max_f32 %1, %0, %2\n\tv_max_f32 %3, %0, %2\n\ts_nop 1\n\tv_permlane32_swap_b32 %1, %3\n\t"
        "v_mov_b32 %0, %1\n\tv_mov_b32 %2, %3\n\ts_nop 0\n\tv_permlane16_swap_b32 %0, %1\n\tv_permlane16_swap_b32 %2, %3"
        : "+v"(mx[0]), "+v"(mx[1]), "+v"(mx[2]), "+v"(mx[3]));
    SB();
    f32x4 sum[4], o0[4], o1[4];
#pragma unroll
    for (int m = 3; m >= 0; --m) {
      const unsigned pa = pk_bf16(__builtin_amdgcn_exp2f(l[m][0] - mx[m]), __builtin_amdgcn_exp2f(l[m][1] - mx[m]));
      const unsigned pb = pk_bf16(__builtin_amdgcn_exp2f(l[m][2] - mx[m]), __builtin_amdgcn_exp2f(l[m][3] - mx[m]));
      const u32x2 p0 = {__builtin_amdgcn_perm(0u, pa, selA), __builtin_amdgcn_perm(0u, pa, selB)}, p1 = {__builtin_amdgcn_perm(0u, pb, selA), __builtin_amdgcn_perm(0u, pb, selB)};
      sum[m] = mfma(ones, cat8u(p0, p1), zero4);
      o0[m] = mfma16(vp[m + 1][0], p1, mfma16(vp[m][0], p0, zero4));
      o1[m] = mfma16(vp[m + 1][1], p1, mfma16(vp[m][1], p0, zero4));
    }
    SB();
    sfor<0, 4>([&](auto m_c) {
      constexpr int m = 3 - decltype(m_c)::v;
      const float inv = __builtin_amdgcn_rcpf(sum[m][0]);
      const u32x2 op0 = pk4(o0[m] * inv), op1 = pk4(o1[m] * inv);
      auto e0 = __builtin_amdgcn_permlane16_swap(op0[0], op1[0], false, false);
      auto e1 = __builtin_amdgcn_permlane16_swap(op0[1], op1[1], false, false);
      st_wt<m * 256>(ost, (u32x4){e0[0], e1[0], e0[1], e1[1]});
    });
    TLR(c);
    TLR(d);
    }
    p2_compute(smem, w, fresh_lane(), r0, tn * 64, tn, x, o, h, hc, ssq, xm, flg + FL_1 + tm * 16 * FS, tok);
    wait_lgkm0(); wait_vm<HW>();
    __builtin_amdgcn_s_setprio(0);
  }
  bar();
  if (w == 0) signal_flag(flg + FL_2 + (pm * 16 + tn) * FS, tok);
  PAGE(10, p14);
  u32x4 hv[4], yv[7];
  char* actw; int yo0, yo1;
  {
    LANE_IDS();
    const int grp = w >> 2, wm = (w >> 1) & 1, wn = w & 1;
    const int r3 = (8 * (tm3 >> 2) + (tm3 & 3)) * 128;
    i32x4 srdA = make_srd(hc, S_ * D_ * 2), srdB = make_srd(grp ? wu : wg, D_ * F_ * 2);
    unsigned voffA = (unsigned)(lane * 16 + (w4 & 1) * 1024 + (w4 >> 1) * CH_);
    const int flip = tn3 >> 4;
    const unsigned sA = (unsigned)r3 * 16;
    auto dmaA = [&](int t, int half, int buf, int j) {
      dma16_dev(lds0 + (half * 2 + buf) * K3_AH + w4 * 1024 + j * 4096, voffA, srdA, sA + (half ^ flip) * 8192 + j * (2 * CH_) + t * KT_);
    };
    unsigned voffB[4];
#pragma unroll
    for (int j = 0; j < 4; ++j) voffB[j] = p3_voffB(j, lane);
    auto dmaB = [&](int t, int buf, int j) {
      if (j < 3 || w4 < 3) dma16(lds0 + K3_B0 + buf * K3_BB + grp * K3_UOFF + w4 * 1024 + j * 4096, voffB[j], srdB, sB3 + (unsigned)(t * 64) * (F_ * 2));
    };
    const int aoff0 = grp * 2 * K3_AH + g * 2048 + (64 * wm + c) * 16;
    const int q4 = c >> 2, p4 = lane & 3;
    const int boffP = K3_B0 + g * 1920 + q4 * 224 + wn * 112 + 8 * p4;
    const int boffM = K3_B0 + g * 1920 + q4 * 224 + wn * 112 + 96 + (p4 >> 1) * K3_UOFF + 8 * (p4 & 1);
    bf16x8 fx[4][2]; s16x4 fw[7][2][2];
    auto rd = [&](int abuf, int bbuf, auto r_c) {
      constexpr int r = decltype(r_c)::v, ks = r / 18, q = r % 18;
      if constexpr (q < 14) {
        constexpr int j = q >> 1, hf = q & 1;
        fw[j][ks][hf] = lds_tr(smem + (j == 6 ? boffM : boffP) + bbuf * K3_BB + (j < 3 ? j * 32 : (j < 6 ? K3_UOFF + (j - 3) * 32 : 0)) + ks * 7680 + hf * 896);
      } else fx[q - 14][ks] = lds_b128(smem + aoff0 + ks * 8192 + abuf * K3_AH + (q - 14) * 256);
    };
    f32x4 acc[4][7];
#pragma unroll
    for (int m = 0; m < 4; ++m)
#pragma unroll
      for (int j = 0; j < 7; ++j) zero_pin(acc[m][j]);
    auto mma = [&]() {
      __builtin_amdgcn_s_setprio(1);
#pragma unroll
      for (int j = 0; j < 7; ++j)
#pragma unroll
        for (int mi = 0; mi < 4; ++mi)
#pragma unroll
          for (int ki = 0; ki < 2; ++ki) { const int m = (j & 1) ? 3 - mi : mi, ks = ki ^ (mi & 1); mfma_ip(acc[m][j], cat8(fw[j][ks][0], fw[j][ks][1]), fx[m][ks]); }
      __builtin_amdgcn_s_setprio(0);
    };
    constexpr int NT = D_ / 64;
#pragma unroll
    for (int j = 0; j < 4; ++j) pin(voffB[j]);
    sfor<0, 36>([&](auto r) { if constexpr (decltype(r)::v % 18 < 14) rd(0, 0, r); });
    SB();
    unsigned la0 = lds0 + grp * 2 * K3_AH + w4 * 1024, so0 = sA + (grp ^ flip) * 8192;
    asm volatile("" : "+s"(srdA), "+s"(srdB), "+s"(la0), "+s"(so0)); pin(voffA);
    Poll pv; poll_regs(pv);
    if (w == PW3) poll_flags(pv, flg + FL_2 + (tm3 * 32 + (lane & 31)) * FS, 0xffffffffull, tok);
    bar();
    acquire_agent();
    TLS(3, 0);
#pragma unroll
    for (int j = 0; j < 4; ++j) dma16_dev(la0 + j * 4096, voffA, srdA, so0 + j * (2 * CH_));
    if (grp == 0) {
      wait_vm<0>(); poll_done(pv); bar();
    } else {
      float st[16];
      const float* sp = ssq + r3 + (w4 >> 1) * 512 + (w4 & 1) * 64 + lane;
#pragma unroll
      for (int i = 0; i < 16; ++i) asm volatile("global_load_dword %0, %1, off sc1" : "=v"(st[i]) : "v"(sp + i * S_));
#pragma unroll
      for (int j = 0; j < 4; ++j) { dmaA(1, 0, 1, j); dmaB(1, 1, j); }
      bar();
      wait_vm<23>(); bar();
      asm volatile("s_waitcnt vmcnt(7)" : "+v"(st[0]), "+v"(st[1]), "+v"(st[2]), "+v"(st[3]), "+v"(st[4]), "+v"(st[5]), "+v"(st[6]), "+v"(st[7]),
                   "+v"(st[8]), "+v"(st[9]), "+v"(st[10]), "+v"(st[11]), "+v"(st[12]), "+v"(st[13]), "+v"(st[14]), "+v"(st[15]) :: "memory");
      float q = 0.f;
#pragma unroll
      for (int i = 0; i < 16; ++i) q += st[i];
      *(LAS float*)(smem + K3_TAB + (w4 * 64 + lane) * 4) = ln_rstd(q * (1.f / D_));
    }
    mfma_fence(); SB();
    const int ahalf = grp ^ 1;
    const int nochk = ((tn ^ (3 - (int)((size_t)wd >> 7))) & 6) | grp;
    sfor<0, NT>([&](auto t_c) {
      constexpr int t = decltype(t_c)::v, ab = t & 1, bb = t % 3;
      const int abuf = ab ^ ahalf;
      if constexpr (t == 1) PAGE(11, p15); else if constexpr (t == 4) PAGE(12, r2); else if constexpr (t == 7) PAGE(13, r2); else if constexpr (t == 10) PAGE(14, r2); else if constexpr (t == 13) PAGE(15, r2);
      sfor<0, 36>([&](auto r) {
        constexpr int rr = decltype(r)::v;
        if constexpr (t > 0 || rr % 18 >= 14) rd(ab, bb, r);
        if constexpr (t < NT - 2 && (rr & 3) == 3 && rr < 32) {
          constexpr int d = rr >> 2;
          if constexpr (d & 1) dmaB(t + 2, (bb + 2) % 3, d >> 1); else dmaA(t + 1 + grp, ahalf, abuf, d >> 1);
        }
        if constexpr (t == NT - 2 && (rr & 7) == 3 && rr < 32) { if (grp == 0) dmaA(t + 1, 1, ab ^ 1, rr >> 3); }
        if constexpr (t < NT - 2) SB();
      });
      if constexpr (t == T_FL)
        asm volatile("s_cmp_lg_u32 %4, 0\n\ts_cbranch_scc1 1f\n\ts_mov_b32 m0, %0\n\ts_nop 0\n\tbuffer_load_dword %1, %2, %3 offen sc1 lds\n1:"
                     :: "s"(lds0 + K3_FL + w4 * 256), "v"((unsigned)lane * (FS * 8)), "s"(make_srd(flg + FL_1, 256 * FS * 8)), "s"(w4 * (64 * FS * 8) + 4), "s"(nochk) : "scc");
      if constexpr (t == T_FL + 2)
        if (nochk == 0 && __builtin_amdgcn_ballot_w64(*(const LAS unsigned*)(smem + K3_FL + w4 * 256 + lane * 4) != tag) != 0) wait_flags(flg + FL_1 + w4 * 64 * FS, 64, tok);
      if constexpr (t == T_CT) dma16_dev_if0(w - 3, lds0 + K3_DUMMY, (unsigned)lane * 4096u, code_srd(), (unsigned)idx * 128u);
      if constexpr (t < 8 && (t & 1))
        dma16(lds0 + K3_DUMMY, lane < 7 ? (unsigned)lane * 128u : OOB, make_srd((w & 1) ? wu : wg, D_ * F_ * 2), (512u + 64u * (t - 1)) * F_ * 2 + (unsigned)(bid * 4 + (w >> 1)) * 896u);
      constexpr int NTW = 1;
      if constexpr (t == T_WD)
        dma16(lds0 + K3_DUMMY, lane < 28 ? (unsigned)lane * 128u : OOB, make_srd(wd, D_ * F_ * 2), (unsigned)bid * 28672u + w * 3584u);
      wait_lgkm0();
      if constexpr (t < NT - 2) wait_vm<7>();
      else if constexpr (t == NT - 2) { if (grp == 0) wait_vm<4 + NTW>(); else wait_vm<NTW>(); }
      else wait_vm<NTW>();
      if constexpr (t == NT - 1) {
#pragma unroll
        for (int i = 0; i < 2; ++i) st_wt(part + ((tm3 * 32 + tn3) * 16 + w * 2 + i) * 64 + lane, (u32x4){~0u, ~0u, ~0u, ~0u});
        if (grp == 0) VISIT4(w4, 16, 17, 21, 19, r4); else VISIT4(w4, 18, 22, 18, 22, r5);
        SB();
      }
      bar(); SB();
      mma();
      SB();
      if constexpr (t < NT - 1) bar(); else { if (grp == 0) bar(); }
      SB();
    });
    mfma_fence(); SB();
    TLS(3, 2);
    PAGE(16, r4);
    if (grp == 0) {
      const i32x4 srdH = make_srd(h, S_ * D_ * 2);
      const unsigned vo = (unsigned)(bid * 16384 + wm * 8192 + wn * 1024 + lane * 16);
#pragma unroll
      for (int m = 0; m < 4; ++m) ldb_async<0>(hv[m], vo, srdH, m * 2048);
    }
    const float C0 = -2.0f * 1.4426950408889634f * 0.7978845608028654f, C1 = C0 * 0.044715f;
    auto geglu2 = [&](f32x2 gt, f32x2 up, float c0a, float c1a, float ia2) {
      const f32x2 z = gt * (gt * gt * c1a + c0a);
      f32x2 e; e.x = __builtin_amdgcn_exp2f(z.x); e.y = __builtin_amdgcn_exp2f(z.y);
      const f32x2 d = e * ia2 + ia2;
      f32x2 r; r.x = __builtin_amdgcn_rcpf(d.x); r.y = __builtin_amdgcn_rcpf(d.y);
      const f32x2 o2 = gt * up * r;
      return pk_bf16(o2.x, o2.y);
    };
    const int rowb = 128 * (grp ^ flip) + 64 * wm;
    actw = (char*)act + (size_t)(14 * tn3 + 7 * wn) * CH_ + (r3 + 512 * (grp ^ flip) + 64 * wm + c) * 16;
    yo0 = (g >> 1) * CH_ + (g & 1) * 256; yo1 = (2 * (g & 1) + (g >> 1)) * 256;
    float c0a[4], c1a[4], ia2[4];
#pragma unroll
    for (int m = 0; m < 4; ++m) {
      const float a = *(const LAS float*)(smem + K3_TAB + (rowb + 16 * m + c) * 4);
      c0a[m] = C0 * a; c1a[m] = C1 * a * a * a; ia2[m] = __builtin_amdgcn_rcpf(a * a);
    }
    auto geglu4 = [&](f32x4 gt, f32x4 up, float p0, float p1, float p2) {
      return (u32x2){geglu2((f32x2){gt[0], gt[1]}, (f32x2){up[0], up[1]}, p0, p1, p2), geglu2((f32x2){gt[2], gt[3]}, (f32x2){up[2], up[3]}, p0, p1, p2)};
    };
    auto out7 = [&](int k, char* p, u32x2 a0, u32x2 a1) {
      auto e0 = __builtin_amdgcn_permlane16_swap(a0[0], a1[0], false, false);
      auto e1 = __builtin_amdgcn_permlane16_swap(a0[1], a1[1], false, false);
      yv[k] = (u32x4){e0[0], e1[0], e0[1], e1[1]};
      if (k < YD || grp == 0) st_wt(p, yv[k]);
    };
#pragma unroll
    for (int mp = 0; mp < 2; ++mp)
#pragma unroll
      for (int b = 0; b < 3; ++b)
        out7(3 * mp + b, actw + 2 * b * CH_ + 2 * mp * 256 + yo0,
             geglu4(acc[2 * mp][b], acc[2 * mp][3 + b], c0a[2 * mp], c1a[2 * mp], ia2[2 * mp]),
             geglu4(acc[2 * mp + 1][b], acc[2 * mp + 1][3 + b], c0a[2 * mp + 1], c1a[2 * mp + 1], ia2[2 * mp + 1]));
    u32x2 mx[2];
#pragma unroll
    for (int mp = 0; mp < 2; ++mp) {
      f32x4 gt, up;
#pragma unroll
      for (int r = 0; r < 4; ++r) {
        auto sv = __builtin_amdgcn_permlane32_swap(__float_as_uint(acc[2 * mp][6][r]), __float_as_uint(acc[2 * mp + 1][6][r]), false, false);
        gt[r] = __uint_as_float(sv[0]); up[r] = __uint_as_float(sv[1]);
      }
      const bool hi = g >> 1;
      mx[mp] = geglu4(gt, up, hi ? c0a[2 * mp + 1] : c0a[2 * mp], hi ? c1a[2 * mp + 1] : c1a[2 * mp], hi ? ia2[2 * mp + 1] : ia2[2 * mp]);
    }
    out7(6, actw + 6 * CH_ + yo1, mx[0], mx[1]);
    TLS(3, 3);
  }
  {
    LANE_IDS();
    P4_DEFS();
    const i32x4 srdA = make_srd(act, S_ * F_ * 2);
    const unsigned voffA = (unsigned)(lane * 16 + (w4 & 1) * 1024 + (w4 >> 1) * CH_), sA = (unsigned)r0 * 16;
    auto issueA = [&](unsigned T, int slot) {
#pragma unroll
      for (int j = 0; j < 4; ++j) dma16_dev(lds0 + slot * 16384 + w4 * 1024 + j * 4096, voffA, srdA, sA + j * (2 * CH_) + T * KT_);
    };
    u64* const f3 = flg + FL_3 + (pm >> 1) * 64 * FS;
    u64* const myf = flg + FL_3 + (tm3 * 64 + ((w >> 2) ^ (tn3 >> 4)) * 32 + tn3) * FS;
    if (w >= 4) {
      PAGE(19, r4);
      constexpr unsigned U = 64 * D_ * 2;
      static_assert(KT_ == 2 * U, "");
      const unsigned lW0 = lds0 + P4_W + w4 * 1024, c0 = tn * 128, c1 = c0 ^ 128, vA = voffA + sA;
      unsigned lW = lW0 + 3 * 16384, lA = lds0 + w4 * 1024 + 16384, oW = tile4(3) * U, nA = tile4(2) * KT_, oA, cnt, t0, t1;
      u64 need = w4 ? 0ull : ((pm & 1) ? 0xffffull << 32 : 0xffffull << 16) | 0x300000003ull << ((pm & 1) * 16 + (tn & 14));
      u64 ret, fv, tokv = tok;
      unsigned dW = dir * (int)U, wf = kh ? 56 * U : 0u - U, wt = kh ? 0u : 55 * U, lWe = lW0 + 5 * 16384;
      unsigned vo[8] = {voffW0 + c0, voffW0 + c0 + U / 2, voffW0 + c1, voffW0 + c1 + U / 2, vA, vA + 2 * CH_, vA + 4 * CH_, vA + 6 * CH_};
      u64* fp = f3 + lane * FS; u64* mf = myf;
      char* yp[7];
#pragma unroll
      for (int k = YD; k < 7; ++k) { yp[k] = k < 6 ? actw + 2 * (k % 3) * CH_ + 2 * (k / 3) * 256 + yo0 : actw + 6 * CH_ + yo1; pin(yp[k]); }
      asm volatile("" : "+s"(lW), "+s"(lA), "+s"(oW), "+s"(nA), "+s"(need), "+s"(dW), "+s"(wf), "+s"(wt), "+s"(lWe), "+v"(fp), "+v"(mf), "+v"(tokv),
                   "+v"(vo[0]), "+v"(vo[1]), "+v"(vo[2]), "+v"(vo[3]), "+v"(vo[4]), "+v"(vo[5]), "+v"(vo[6]), "+v"(vo[7]));
      bar();
#pragma unroll
      for (int k = YD; k < 7; ++k) st_wt(yp[k], yv[k]);
#define P4_DMA(v, srd, o) "buffer_load_dwordx4 %[" #v "], %[" #srd "], %[" #o "] offen sc1 lds\n\t"
      asm volatile(
        "s_mov_b64 vcc, 0\n\t"
        "s_mov_b32 %[oA], %[nA]\n\ts_add_u32 %[lA], %[lA], 0x4000\n\ts_movk_i32 %[cnt], 1\n\ts_call_b64 %[ret], 4f\n\t"
        "s_movk_i32 %[cnt], 1\n\ts_call_b64 %[ret], 3f\n\t"
        "s_movk_i32 %[cnt], 2\n\ts_call_b64 %[ret], 2f\n\t"
        "v_mov_b32 %[t0], %[lsync]\n\tv_mov_b32 %[t1], 1\n\ts_mov_b64 exec, 1\n\tds_add_rtn_u32 %[t0], %[t0], %[t1]\n\ts_waitcnt lgkmcnt(0)\n\t"
        "v_cmp_eq_u32 %[ret], 3, %[t0]\n\ts_mov_b64 exec, %[ret]\n\tglobal_store_dwordx2 %[myf], %[tok], off sc1\n\ts_mov_b64 exec, -1\n\t"
        "s_movk_i32 %[cnt], 17\n\ts_cmp_eq_u64 %[need], 0\n\ts_cbranch_scc1 5f\n\t"
        "s_movk_i32 %[cnt], 6\n\ts_call_b64 %[ret], 1f\n\t"
        "s_mov_b64 exec, %[need]\n\tglobal_load_dwordx2 %[fv], %[fp], off sc1\n\ts_mov_b64 exec, -1\n\t"
        "s_movk_i32 %[cnt], 2\n\ts_call_b64 %[ret], 1f\n\t"
        "s_waitcnt vmcnt(16)\n\ts_movk_i32 %[cnt], 0x7fff\n"
        "6:\n\tv_cmp_ne_u64 %[ret], %[tok], %[fv]\n\ts_and_b64 %[ret], %[ret], %[need]\n\ts_cbranch_scc0 7f\n\ts_sub_u32 %[cnt], %[cnt], 1\n\ts_cbranch_scc1 7f\n\t"
        "s_sleep 1\n\ts_mov_b64 exec, %[need]\n\tglobal_load_dwordx2 %[fv], %[fp], off sc1\n\ts_mov_b64 exec, -1\n\ts_waitcnt vmcnt(0)\n\ts_branch 6b\n"
        "7:\n\ts_movk_i32 %[cnt], 9\n"
        "5:\n\ts_call_b64 %[ret], 1f\n\t"
        "s_mov_b64 vcc, -1\n\ts_movk_i32 %[cnt], 8\n\ts_call_b64 %[ret], 1f\n\t"
        "s_branch 9f\n"
        "0:\n\ts_waitcnt vmcnt(12)\n"
        "1:\n\ts_waitcnt vmcnt(16)\n"
        "2:\n\ts_barrier\n"
        "3:\n\ts_mov_b32 m0, %[lW]\n\ts_mov_b32 %[oA], %[nA]\n\t" P4_DMA(w0, sW, oW)
        "s_add_u32 m0, %[lW], 0x1000\n\ts_add_u32 %[lA], %[lA], 0x4000\n\t" P4_DMA(w1, sW, oW)
        "s_cbranch_vccnz 4f\n\t"
        "s_add_u32 m0, %[lW], 0x2000\n\ts_nop 0\n\t" P4_DMA(w2, sW1, oW)
        "s_add_u32 m0, %[lW], 0x3000\n\ts_nop 0\n\t" P4_DMA(w3, sW1, oW)
        "\n4:\n\ts_and_b32 %[lA], %[lA], 0xffff\n\ts_mov_b32 m0, %[lA]\n\ts_lshl_b32 %[nA], %[oW], 1\n\t" P4_DMA(a0, sA, oA)
        "s_add_u32 m0, %[lA], 0x1000\n\ts_add_u32 %[oW], %[oW], %[dW]\n\t" P4_DMA(a1, sA, oA)
        "s_cmp_eq_u32 %[oW], %[wf]\n\ts_cselect_b32 %[oW], %[wt], %[oW]\n\t"
        "s_add_u32 m0, %[lA], 0x2000\n\ts_add_u32 %[lW], %[lW], 0x4000\n\t" P4_DMA(a2, sA, oA)
        "s_cmp_eq_u32 %[lW], %[lWe]\n\ts_cselect_b32 %[lW], %[lW0], %[lW]\n\t"
        "s_add_u32 m0, %[lA], 0x3000\n\ts_sub_u32 %[cnt], %[cnt], 1\n\t" P4_DMA(a3, sA, oA)
        "s_cmp_eq_u32 %[cnt], 0\n\ts_cbranch_scc1 8f\n\ts_cbranch_vccz 1b\n\ts_branch 0b\n"
        "8:\n\ts_setpc_b64 %[ret]\n"
        "9:"
        : [lW] "+s"(lW), [lA] "+s"(lA), [oW] "+s"(oW), [nA] "+s"(nA), [oA] "=&s"(oA), [cnt] "=&s"(cnt), [ret] "=&s"(ret), [fv] "=&v"(fv), [t0] "=&v"(t0), [t1] "=&v"(t1)
        : [sW] "s"(srdW), [sW1] "s"(srdW1), [sA] "s"(srdA), [dW] "s"(dW), [wf] "s"(wf), [wt] "s"(wt), [lW0] "s"(lW0), [lWe] "s"(lWe), [need] "s"(need),
          [lsync] "n"(L_SYNC + 8), [w0] "v"(vo[0]), [w1] "v"(vo[1]), [w2] "v"(vo[2]), [w3] "v"(vo[3]), [a0] "v"(vo[4]), [a1] "v"(vo[5]), [a2] "v"(vo[6]),
          [a3] "v"(vo[7]), [fp] "v"(fp), [myf] "v"(mf), [tok] "v"(tokv)
        : "vcc", "scc", "memory");
      wait_vm<12>(); bar();
#pragma unroll
      for (int j = 0; j < 4; ++j) dma16_dev(lds0 + 3 * 16384 + w4 * 1024 + j * 4096, vo[4 + j], srdA, nA);
      auto inbox = [&]() {
#pragma unroll
        for (int j = 0; j < 4; ++j) dma16_dev(lds0 + P4_INB + (w4 * 4 + j) * 1024, (unsigned)lane * 16u, make_srd(part + (pm * 16 + tn) * 1024, 16384), (unsigned)(w4 * 4 + j) * 1024u);
      };
      wait_vm<10>(); bar();
      if constexpr (P4_IN == 4) inbox();
      wait_vm<4 + (P4_IN == 4 ? 4 : 0)>(); bar();
      if constexpr (P4_IN == 3) inbox();
      wait_vm<(P4_IN >= 3 ? 4 : 0)>(); bar();
      if constexpr (P4_IN == 2) inbox();
      wait_vm<0>(); bar();
      if (bid == 0 && w == 7) {
#pragma unroll
        for (int i = 0; i < 4; ++i) wait_flags(flg + FL_1 + 64 * i * FS, 64, tok);
        signal_flag(flg, tok);
      }
      asm volatile(".Lkm_last:");
    } else {
      const int wm = w >> 1, wo_ = w & 1;
      PAGE(17, r4);
      const int aoff0 = g * 2048 + (64 * wm + c) * 16;
      const int q4 = c >> 2, p4 = lane & 3, fR = ((q4 >> 1) & 1) | ((g & 1) << 1);
      const int woff0 = P4_W + (8 * g + q4) * 128 + ((fR ^ (2 * wo_)) << 5) + 8 * p4;
      int wb[4] = {woff0, woff0 ^ 32, woff0 + 8192, (woff0 ^ 32) + 8192}, ws5 = 0;
      bf16x8 fx[4][2]; s16x4 fw[4][2][2];
      auto rdx = [&](int slot, int m, int ks) { fx[m][ks] = lds_b128(smem + slot * 16384 + aoff0 + ks * 8192 + m * 256); };
      auto rdw = [&](int j, int q) { fw[j][q >> 1][q & 1] = lds_tr(smem + wb[j] + (q >> 1) * 4096 + (q & 1) * 512); };
      asm volatile("s_waitcnt vmcnt(0)" : "+v"(hv[0]), "+v"(hv[1]), "+v"(hv[2]), "+v"(hv[3]) :: "memory");
      unsigned rank = 0;
      if (lane == 0) rank = __hip_atomic_fetch_add((LAS unsigned*)(smem + L_SYNC + 4), 1u, __ATOMIC_RELAXED, __HIP_MEMORY_SCOPE_WORKGROUP);
      rank = __builtin_amdgcn_readfirstlane(rank);
      if (rank == 3) signal_flag(myf, tok);
      TLS(7, 0);
#pragma unroll
      for (int s = 0; s < NWX; ++s) issueW(tile4(s), s, true);
      SB();
      f32x4 acc[4][4];
#pragma unroll
      for (int m = 0; m < 4; ++m) {
        auto e0 = __builtin_amdgcn_permlane16_swap(hv[m][0], hv[m][2], false, false);
        auto e1 = __builtin_amdgcn_permlane16_swap(hv[m][1], hv[m][3], false, false);
#pragma unroll
        for (int j = 0; j < 2; ++j) { acc[m][j] = (f32x4){bf_lo(e0[j]), bf_hi(e0[j]), bf_lo(e1[j]), bf_hi(e1[j])}; pin(acc[m][j]); zero_pin(acc[m][2 + j]); }
      }
      __builtin_amdgcn_s_setprio(1);
      wait_flags(f3 + ((pm & 1) * 48 + kh * 8) * FS, 8, tok);
      acquire_agent();
      TLS(4, 0);
#pragma unroll
      for (int s = 0; s < 2; ++s) issueA(tile4(s), s);
      SB();
      PAGE(21, r4);
      if (lane == 0) __hip_atomic_fetch_add((LAS unsigned*)(smem + L_SYNC + 20), 1u, __ATOMIC_RELAXED, __HIP_MEMORY_SCOPE_WORKGROUP);
      NOUNROLL for (unsigned spins = 0; spins < SPIN_MAX * 16 && __builtin_amdgcn_readfirstlane(*(LAS volatile int*)(smem + L_SYNC + 20)) < 4; ++spins) __builtin_amdgcn_s_sleep(1);
      acquire_agent();
      auto mm = [&](int hf, auto i_c) {
        constexpr int i = decltype(i_c)::v, m = i >> 2, jj = ((i >> 1) ^ m) & 1, ks = (i ^ (i >> 1)) & 1;
        mfma_ip(acc[m][2 * hf + jj], cat8(fw[2 * hf + jj][ks][0], fw[2 * hf + jj][ks][1]), fx[m][ks]);
      };
#pragma unroll
      for (int r = 0; r < 8; ++r) rdw(r >> 2, r & 3);
      SB();
      wait_vm<4>(); bar();
      TLS(4, 1);
#pragma unroll
      for (int r = 0; r < 4; ++r) rdx(0, r >> 1, r & 1);
      mfma_fence(); SB();
#pragma clang loop unroll(disable)
      for (int it = 0; it < NX / 4; ++it) {
        sfor<0, 4>([&](auto s_c) {
          constexpr int s = decltype(s_c)::v, NB = 3;
          const int dw = ws5 == 4 ? -4 * 16384 : 16384;
          ws5 = ws5 == 4 ? 0 : ws5 + 1;
          sfor<0, 16>([&](auto i_c) {
            constexpr int i = decltype(i_c)::v;
            mm(0, i_c);
            if constexpr (i < 4) rdx(s, 2 + (i >> 1), i & 1);
            else if constexpr (i < 12) rdw(2 + ((i - 4) >> 2), i & 3);
            else { wb[i - 12] += dw; pin(wb[i - 12]); }
            SB();
          });
          sfor<0, NB>([&](auto i) { mm(1, i); SB(); });
          if constexpr (s == 0) wait_vm<0>();
          wait_lgkm0(); bar(); SB();
          sfor<NB, 16>([&](auto i_c) {
            constexpr int i = decltype(i_c)::v;
            mm(1, i_c);
            if constexpr (i < 7) rdw(0, i - 3);
            else if constexpr (i < 9) rdx((s + 1) & 3, 0, i - 7);
            else if constexpr (i < 13) rdw(1, i - 9);
            else if constexpr (i < 15) rdx((s + 1) & 3, 1, i - 13);
            SB();
          });
        });
      }
      PAGE(18, r5);
      SB();
      auto rdt = [&](int s, int q) {
        fw[2 * (s & 1) + (q >> 2)][(q >> 1) & 1][q & 1] = lds_tr(smem + (woff0 ^ ((q >> 2) << 5)) + (s % 5) * 16384 + ((q >> 1) & 1) * 4096 + (q & 1) * 512);
      };
      u32x4* const pw = part + ((pm * 16 + (tn ^ 1)) * 16 + w * 4) * 64 + lane;
      sfor<NX, NS4>([&](auto s_c) {
        constexpr int s = decltype(s_c)::v;
        sfor<0, 16>([&](auto i_c) {
          constexpr int i = decltype(i_c)::v, m = i >> 2, jj = ((i >> 1) ^ m) & 1, ks = (i ^ (i >> 1)) & 1;
          if constexpr (i == TB && s == NS4 - 3) PAGE(22, r5);
          if constexpr (i == TB) { wait_lgkm0(); bar(); SB(); }
          mfma_ip(acc[m][jj], cat8(fw[2 * (s & 1) + jj][ks][0], fw[2 * (s & 1) + jj][ks][1]), fx[m][ks]);
          if constexpr (i < 4) rdx(s & 3, 2 + (i >> 1), i & 1);
          else if constexpr (s == NX && i < 8) {
            const u32x2 a0 = pk4(acc[i - 4][2]), a1 = pk4(acc[i - 4][3]);
            st_wt(pw + (i - 4) * 64, __builtin_elementwise_min((u32x4){a0[0], a0[1], a1[0], a1[1]}, (u32x4){~1u, ~1u, ~1u, ~1u}));
          } else if constexpr (i >= TB && s < NS4 - 1) {
            constexpr int j = i - TB, n2 = 12 - (16 - TB);
            auto rd = [&](auto r_c) { constexpr int r = decltype(r_c)::v; if constexpr (r < 8) rdt(s + 1, r); else rdx((s + 1) & 3, (r - 8) >> 1, r & 1); };
            if constexpr (j < n2) { rd(IC<2 * j>{}); rd(IC<2 * j + 1>{}); } else rd(IC<j + n2>{});
          }
          SB();
        });
      });
      mfma_fence(); SB();
      {
        TLS(4, 2);
        u32x4 pp[4];
        unsigned mx = 0;
#pragma unroll
        for (int i = 0; i < 4; ++i) {
          pp[i] = *(const LAS u32x4*)(smem + P4_INB + (w * 4 + i) * 1024 + lane * 16);
          mx = umax(umax(umax(mx, pp[i][0]), pp[i][1]), umax(pp[i][2], pp[i][3]));
        }
        NOUNROLL for (unsigned spins = 0; spins < SPIN_MAX && __builtin_amdgcn_ballot_w64(mx == ~0u) != 0; ++spins) {
#pragma unroll
          for (int i = 0; i < 4; ++i) ld_async_dev(pp[i], part + ((pm * 16 + tn) * 16 + w * 4 + i) * 64 + lane);
          asm volatile("s_waitcnt vmcnt(0)" : "+v"(pp[0]), "+v"(pp[1]), "+v"(pp[2]), "+v"(pp[3]) :: "memory");
          mx = 0;
#pragma unroll
          for (int i = 0; i < 4; ++i) mx = umax(umax(umax(mx, pp[i][0]), pp[i][1]), umax(pp[i][2], pp[i][3]));
        }
        char* const op = (char*)out + (size_t)(r0 + 64 * wm + c) * (D_ * 2) + tn * 128 + wo_ * 64 + ((g & 1) << 5) + ((g >> 1) << 4);
#pragma unroll
        for (int m = 0; m < 4; ++m) {
          const u32x4 q = pp[m];
          const u32x2 a0 = pk4(acc[m][0] + (f32x4){bf_lo(q[0]), bf_hi(q[0]), bf_lo(q[1]), bf_hi(q[1])});
          const u32x2 a1 = pk4(acc[m][1] + (f32x4){bf_lo(q[2]), bf_hi(q[2]), bf_lo(q[3]), bf_hi(q[3])});
          auto e0 = __builtin_amdgcn_permlane16_swap(a0[0], a1[0], false, false);
          auto e1 = __builtin_amdgcn_permlane16_swap(a0[1], a1[1], false, false);
          st_wt(op + m * (16 * D_ * 2), (u32x4){e0[0], e1[0], e0[1], e1[1]});
        }
        TLS(4, 3);
      }
    }
  }
}

extern "C" void kernel_launch(void* const* d_in, const int* in_sizes, int n_in, void* d_out, int out_size, void* d_ws, size_t ws_size,
                              hipStream_t stream) {
  const u16* x = (const u16*)d_in[0];
  const u16* wqkv = (const u16*)d_in[1];
  const u16* wo = (const u16*)d_in[2];
  const u16* wg = (const u16*)d_in[4];
  const u16* wu = (const u16*)d_in[5];
  const u16* wd = (const u16*)d_in[6];
  char* ws = (char*)d_ws;
  km_block<<<256, 512, 0, stream>>>(x, wqkv, wo, wg, wu, wd, (u16*)d_out, ws);
}
```
